```python
import torch

def _softmax(x, axis=-1):
    return torch.softmax(x, dim=axis)
def _gelu(x):
    return torch.nn.functional.gelu(x, approximate="tanh")
def _silu(x):
    return torch.nn.functional.silu(x)
def _sigmoid(x):
    return torch.sigmoid(x)
def _layernorm(x, axis=-1, eps=1e-5):
    m = x.mean(dim=axis, keepdim=True)
    v = x.var(dim=axis, keepdim=True, unbiased=False)
    return (x - m) / torch.sqrt(v + eps)
def _scatter_add(tgt, idx, src, axis=0):
    return tgt.clone().index_add_(axis, idx, src)
def _segment_sum(src, seg, n):
    out = torch.zeros((n,) + src.shape[1:], dtype=src.dtype)
    return out.index_add_(0, seg, src)
def _pad(x, pw):
    flat = tuple(v for pair in reversed(pw) for v in pair)
    return torch.nn.functional.pad(x, flat)

kernel_name = 'concat_heads__ssm_conv_h3__spectral_fnet__prompt'

def setup_inputs():

    _w = (lambda a: (lambda t: t.to(torch.bfloat16) if t.is_floating_point() else t)(torch.as_tensor(a)))
    def _A_setup():
        B, S, D = 1, 2048, 640
        ks = 4
        return {
            "q": _w((lambda *s: torch.randn(*s, dtype=torch.bfloat16) * 0.05)(B, S, D)),
            "k": _w((lambda *s: torch.randn(*s, dtype=torch.bfloat16) * 3.2)(B, S, D)),
            "v": _w((lambda *s: torch.randn(*s, dtype=torch.bfloat16) * 3.2)(B, S, D)),
            "shift_k": _w((lambda *s: torch.randn(*s, dtype=torch.bfloat16) * 3.2)(D, ks) * 0.5),
            "ssm_k": _w((lambda *s: torch.randn(*s, dtype=torch.bfloat16) * 3.2)(D, S) * (1.0 / S ** 0.5)),
            "ssm_D": _w((lambda *s: torch.randn(*s, dtype=torch.bfloat16) * 3.2)(D)),
        }
    _A_in = _A_setup()

    def _B_setup():
        B, S, D = 1, 2048, 640
        ns = torch.arange(S)[:, None] * torch.arange(S)[None, :]
        nd = torch.arange(D)[:, None] * torch.arange(D)[None, :]
        Ws_re = torch.cos(-2.0 * torch.pi * ns / S).to(float)
        Ws_im = torch.sin(-2.0 * torch.pi * ns / S).to(float)
        Wd_re = torch.cos(-2.0 * torch.pi * nd / D).to(float)
        Wd_im = torch.sin(-2.0 * torch.pi * nd / D).to(float)
        return {
            "x": _w((lambda *s: torch.randn(*s, dtype=torch.bfloat16) * 0.05)(B, S, D)),
            "dft_seq_re": _w(Ws_re),
            "dft_seq_im": _w(Ws_im),
            "dft_hid_re": _w(Wd_re),
            "dft_hid_im": _w(Wd_im),
        }
    _B_in = _B_setup()

    return {
        "A_q": _A_in["q"],
        "A_k": _A_in["k"],
        "A_v": _A_in["v"],
        "A_shift_k": _A_in["shift_k"],
        "A_ssm_k": _A_in["ssm_k"],
        "A_ssm_D": _A_in["ssm_D"],
        "B_x": _B_in["x"],
        "B_dft_seq_re": _B_in["dft_seq_re"],
        "B_dft_seq_im": _B_in["dft_seq_im"],
        "B_dft_hid_re": _B_in["dft_hid_re"],
        "B_dft_hid_im": _B_in["dft_hid_im"],
    }

def reference(A_q, A_k, A_v, A_shift_k, A_ssm_k, A_ssm_D, B_x, B_dft_seq_re, B_dft_seq_im, B_dft_hid_re, B_dft_hid_im):
    def _A_ref(q, k, v, shift_k, ssm_k, ssm_D):
        B, S, D = q.shape
        ks = shift_k.shape[-1]
        k_pad = _pad(k, ((0, 0), (ks - 1, 0), (0, 0)))
        k_shift = torch.zeros_like(k)
        for t in range(ks):
            k_shift = k_shift + k_pad[:, t:t + S, :] * shift_k[:, ks - 1 - t][None, None, :]
        kv = k_shift * v
        L = 2 * S
        kv_pad = _pad(kv, ((0, 0), (0, S), (0, 0)))
        kf_pad = _pad(ssm_k, ((0, 0), (0, S)))
        kv_f = (lambda x, axes=(-2,-1): torch.fft.rfft2(x.float(), dim=axes))(kv_pad[:, None, :, :], axes=(1, 2))
        kf_f = (lambda x, axes=(-2,-1): torch.fft.rfft2(x.float(), dim=axes))(kf_pad[:, None, :], axes=(1, 2))
        kf_f = (lambda x, *d: x.permute(*d))(kf_f, 1, 2, 0)
        y_f = kv_f * kf_f[None, :, :, :]
        y = (lambda x, s, axes=(-2,-1): torch.fft.irfft2(x, s=s, dim=axes).to(torch.bfloat16))(y_f, (1, L), axes=(1, 2))[:, 0, :S, :]
        y = y + kv * ssm_D[None, None, :]
        return q * y

    def _B_ref(x, dft_seq_re, dft_seq_im, dft_hid_re, dft_hid_im):
        a = torch.einsum("bsd,ed->bse", x, dft_hid_re)
        b = torch.einsum("bsd,ed->bse", x, dft_hid_im)
        y = torch.einsum("ts,bse->bte", dft_seq_re, a) - torch.einsum("ts,bse->bte", dft_seq_im, b)
        return y

    a = _A_ref(A_q, A_k, A_v, A_shift_k, A_ssm_k, A_ssm_D)
    b = _B_ref(B_x, B_dft_seq_re, B_dft_seq_im, B_dft_hid_re, B_dft_hid_im)
    return torch.concatenate([a, b], axis=-1)
```

```cpp
#include <hip/hip_runtime.h>
#include <hip/hip_bf16.h>
#include <stdint.h>

typedef __attribute__((ext_vector_type(8))) short bf16x8;
typedef __attribute__((ext_vector_type(4))) short s16x4;
typedef __attribute__((ext_vector_type(4))) float f32x4;
typedef __attribute__((ext_vector_type(4))) unsigned u32x4;
typedef __attribute__((ext_vector_type(2))) unsigned u32x2;
typedef unsigned short u16;

#define LDS_AS __attribute__((address_space(3)))

static constexpr int S_LEN = 2048;
static constexpr int D_DIM = 640;

__device__ __forceinline__ float bf2f(unsigned h) { return __uint_as_float(h << 16); }
__device__ __forceinline__ unsigned f2bf(float f) {
  __hip_bfloat16 b = __float2bfloat16(f);
  return (unsigned)__builtin_bit_cast(unsigned short, b);
}
__device__ __forceinline__ float rbf(float f) { return bf2f(f2bf(f)); }
__device__ __forceinline__ unsigned pack2(float lo, float hi) { return f2bf(lo) | (f2bf(hi) << 16); }

#define WAIT_V(n) asm volatile("s_waitcnt vmcnt(" #n ") lgkmcnt(0)" ::: "memory")
#define BAR() __builtin_amdgcn_s_barrier()
#define SCHED() __builtin_amdgcn_sched_barrier(0)
#define SGB(mask, n) __builtin_amdgcn_sched_group_barrier(mask, n, 0)
#define ILV16() do { _Pragma("unroll") for (int _i = 0; _i < 16; ++_i) { SGB(0x100, 1); SGB(0x8, 1); } } while (0)
#define ILV12_10() do { _Pragma("unroll") for (int _i = 0; _i < 10; ++_i) { SGB(0x100, 1); SGB(0x8, 1); } SGB(0x100, 2); } while (0)

__device__ __forceinline__ void glds16(const void* sbase, unsigned voff, unsigned lds_dst) {
  unsigned keep;
  asm volatile(
      "s_mov_b32 %0, m0\n\ts_mov_b32 m0, %3\n\ts_nop 0\n\tglobal_load_lds_dwordx4 %1, %2\n\ts_mov_b32 m0, %0"
      : "=&s"(keep)
      : "v"(voff), "s"(sbase), "s"(lds_dst)
      : "memory");
}

static constexpr int G1_NS = 5;
static constexpr int G1_CT = 128 * 128;
static constexpr int G1_XT = 64 * 256;
static constexpr int G1_STAGE = G1_CT + G1_XT;
static constexpr int G1_STEPS = S_LEN / 64;
static constexpr int NGEMM1 = 160;
static constexpr int NCONV = 80;

struct G1Frags {
  bf16x8 cf[2][4];
  bf16x8 xf[2][4];
};

__device__ __forceinline__ void big_gemm(char* smem, int b, const u16* __restrict__ Cre,
                                         const u16* __restrict__ Cim, const u16* __restrict__ x,
                                         u16* __restrict__ PQ) {
  const int tid = threadIdx.x;
  const int w = __builtin_amdgcn_readfirstlane(tid >> 6);
  const int lane = tid & 63;
  const int xcd = b & 7, idx = b >> 3;
  const int ttile = xcd * 4 + idx / 5;
  const int dtile = idx % 5;
  const int t0 = ttile * 128;
  const int is_im = t0 >= S_LEN;
  const int trow0 = t0 & (S_LEN - 1);
  const int d0 = dtile * 128;
  auto nxt = [](int bb) { return (bb + 1 == G1_NS) ? 0 : bb + 1; };
  const int g = lane >> 4, r16 = lane & 15;

  if (w >= 4) {
    const int lw = w - 4;
    const u16* Cp = (is_im ? Cim : Cre) + (size_t)trow0 * S_LEN;
    const u16* xp = x + d0;
    const unsigned lds0 = __builtin_amdgcn_readfirstlane((unsigned)(size_t)smem);
    unsigned coff[4], xoff[4];
#pragma unroll
    for (int i = 0; i < 4; ++i) {
      int row = 32 * lw + 8 * i + (lane >> 3);
      int sc = (lane & 7) ^ ((row >> 1) & 7);
      coff[i] = (row * S_LEN + 8 * sc) * 2;
      int xr = 16 * lw + 4 * i + (lane >> 4);
      int r3 = (((xr >> 3) & 1) << 2) | (xr & 3);
      int xc = (lane & 15) ^ (r3 << 1);
      xoff[i] = (xr * D_DIM + 8 * xc) * 2;
    }
    auto stage_issue = [&](int step, int buf) {
      unsigned sb = lds0 + buf * G1_STAGE;
      const u16* cg = Cp + step * 64;
      const u16* xg = xp + (size_t)step * 64 * D_DIM;
#pragma unroll
      for (int i = 0; i < 4; ++i) {
        glds16(cg, coff[i], sb + (32 * lw + 8 * i) * 128);
        glds16(xg, xoff[i], sb + G1_CT + (16 * lw + 4 * i) * 256);
      }
    };
#pragma unroll
    for (int s = 0; s < G1_NS; ++s) stage_issue(s, s);
    WAIT_V(32);
    BAR();
    int bk = 0;
    for (int k = 0; k < 27; ++k) {
      WAIT_V(24);
      BAR();
      stage_issue(k + G1_NS, bk);
      bk = nxt(bk);
    }
    WAIT_V(24); BAR();
    WAIT_V(16); BAR();
    WAIT_V(8);  BAR();
    WAIT_V(0);  BAR();
  } else {
    const int wt = w >> 1, wd = w & 1;
    int crd[2];
#pragma unroll
    for (int ks = 0; ks < 2; ++ks)
      crd[ks] = (64 * wt + r16) * 128 + 16 * ((4 * ks + g) ^ ((r16 >> 1) & 7));
    const int q4 = (lane & 15) >> 2, p4 = lane & 3;
    int xrd[4];
#pragma unroll
    for (int dt = 0; dt < 4; ++dt)
      xrd[dt] = G1_CT + (8 * g + q4) * 256 + 32 * ((4 * wd + dt) ^ (((g & 1) << 2) | q4)) + 8 * p4;

    f32x4 acc[4][4];
#pragma unroll
    for (int i = 0; i < 4; ++i)
#pragma unroll
      for (int j = 0; j < 4; ++j) acc[i][j] = (f32x4){0.f, 0.f, 0.f, 0.f};

    auto load_frags = [&](G1Frags& f, int buf) {
      const char* sb = smem + buf * G1_STAGE;
#pragma unroll
      for (int ks = 0; ks < 2; ++ks) {
#pragma unroll
        for (int dt = 0; dt < 4; ++dt) {
          s16x4 lo = __builtin_amdgcn_ds_read_tr16_b64_v4i16((LDS_AS s16x4*)(sb + xrd[dt] + (32 * ks) * 256));
          s16x4 hi = __builtin_amdgcn_ds_read_tr16_b64_v4i16((LDS_AS s16x4*)(sb + xrd[dt] + (32 * ks + 4) * 256));
          f.xf[ks][dt] = __builtin_shufflevector(lo, hi, 0, 1, 2, 3, 4, 5, 6, 7);
        }
#pragma unroll
        for (int tt = 0; tt < 4; ++tt) f.cf[ks][tt] = *(const bf16x8*)(sb + crd[ks] + tt * 2048);
      }
    };
    auto mma = [&](const G1Frags& f) {
#pragma unroll
      for (int ks = 0; ks < 2; ++ks)
#pragma unroll
        for (int dt = 0; dt < 4; ++dt)
#pragma unroll
          for (int tt = 0; tt < 4; ++tt)
            acc[dt][tt] = __builtin_amdgcn_mfma_f32_16x16x32_bf16(f.xf[ks][dt], f.cf[ks][tt], acc[dt][tt], 0, 0, 0);
    };
#define WAIT_L() asm volatile("s_waitcnt lgkmcnt(0)" ::: "memory")
#define ILV24_32() do { _Pragma("unroll") for (int _i = 0; _i < 24; ++_i) { SGB(0x100, 1); SGB(0x8, 1); } SGB(0x8, 8); } while (0)
#define G1_STEP(FA, FB) \
  do { WAIT_L(); BAR(); int b1 = nxt(bk); SCHED(); load_frags(FB, b1); mma(FA); ILV24_32(); SCHED(); bk = b1; } while (0)
    G1Frags fa, fb;
    BAR();
    load_frags(fa, 0);
    int bk = 0;
    for (int k = 0; k < 30; k += 2) {
      G1_STEP(fa, fb);
      G1_STEP(fb, fa);
    }
    G1_STEP(fa, fb);
    mma(fb);
    WAIT_L();
    const float sgn = is_im ? -1.f : 1.f;
    char* ob = smem + ((bk >= 2) ? 0 : 3 * G1_STAGE);
#pragma unroll
    for (int tt = 0; tt < 4; ++tt)
#pragma unroll
      for (int dt = 0; dt < 4; ++dt) {
        u32x2 v;
        v.x = pack2(sgn * acc[dt][tt][0], sgn * acc[dt][tt][1]);
        v.y = pack2(sgn * acc[dt][tt][2], sgn * acc[dt][tt][3]);
        *(u32x2*)(ob + (64 * wt + 16 * tt + r16) * 272 + (64 * wd + 16 * dt + 4 * g) * 2) = v;
      }
  }
  __syncthreads();
  {
    const char* ob = smem + 3 * G1_STAGE;
    u16* outp = PQ + (size_t)trow0 * (2 * D_DIM) + (is_im ? D_DIM : 0) + d0;
#pragma unroll
    for (int i = 0; i < 4; ++i) {
      int row = 16 * w + 4 * i + g;
      u32x4 v = *(const u32x4*)(ob + row * 272 + r16 * 16);
      *(u32x4*)(outp + (size_t)row * (2 * D_DIM) + r16 * 8) = v;
    }
  }
}

static constexpr int CV_XS = (256 + 2048) * 2;
static constexpr int CV_HC = 2080 * 2;
static constexpr int CV_WAVE = CV_XS + 2 * CV_HC;

typedef __attribute__((ext_vector_type(2))) float f32x2;
typedef __attribute__((ext_vector_type(2))) __bf16 bf16x2v;
__device__ __forceinline__ unsigned cvtpk(f32x2 v) {
  bf16x2v b = __builtin_convertvector(v, bf16x2v);
  return __builtin_bit_cast(unsigned, b);
}
__device__ __forceinline__ f32x2 unpk(unsigned u) {
  f32x2 r;
  r.x = __uint_as_float(u << 16);
  r.y = __uint_as_float(u & 0xffff0000u);
  return r;
}
__device__ __forceinline__ f32x2 unpk_sw(unsigned u) {
  f32x2 r;
  r.x = __uint_as_float(u & 0xffff0000u);
  r.y = __uint_as_float(u << 16);
  return r;
}
__device__ __forceinline__ f32x2 rn2(f32x2 v) { return unpk(cvtpk(v)); }
__device__ __forceinline__ unsigned dw(const u32x4& v, int i) {
  return (i == 0) ? v.x : (i == 1) ? v.y : (i == 2) ? v.z : v.w;
}

__device__ __forceinline__ void conv_block(char* smem, int cb, const u16* __restrict__ q,
                                           const u16* __restrict__ k, const u16* __restrict__ v,
                                           const u16* __restrict__ shk, const u16* __restrict__ ssmk,
                                           const u16* __restrict__ ssmD, u16* __restrict__ out) {
  const int tid = threadIdx.x;
  const int w = __builtin_amdgcn_readfirstlane(tid >> 6);
  const int lane = tid & 63;
  const int grp = (cb & 7) * 10 + (cb >> 3);
  const int d0 = grp * 8;

  const int r0 = 4 * tid;
  u32x4 kr[4], vr[4], hv[4];
#pragma unroll
  for (int r = 0; r < 4; ++r) kr[r] = *(const u32x4*)(k + (size_t)(r0 + r) * D_DIM + d0);
  u32x4 kh[3];
#pragma unroll
  for (int j = 0; j < 3; ++j) {
    if (w == 0) kh[j] = (u32x4){0u, 0u, 0u, 0u};
    else kh[j] = *(const u32x4*)(k + (size_t)(256 * w - 3 + j) * D_DIM + d0);
  }
#pragma unroll
  for (int r = 0; r < 4; ++r) vr[r] = *(const u32x4*)(v + (size_t)(r0 + r) * D_DIM + d0);
  {
    const u16* hg = ssmk + (size_t)(d0 + w) * S_LEN;
#pragma unroll
    for (int i = 0; i < 4; ++i) hv[i] = *(const u32x4*)(hg + 512 * i + 8 * lane);
  }

  {
#pragma unroll
    for (int j = 0; j < 3; ++j) {
      u32x4 t;
      t.x = __shfl_up(kr[1 + j].x, 1, 64);
      t.y = __shfl_up(kr[1 + j].y, 1, 64);
      t.z = __shfl_up(kr[1 + j].z, 1, 64);
      t.w = __shfl_up(kr[1 + j].w, 1, 64);
      if (lane != 0) kh[j] = t;
    }
    const unsigned pos = (256 + (r0 & ~7) + 4 - (r0 & 7)) * 2;
#pragma unroll
    for (int cp = 0; cp < 4; ++cp) {
      u32x4 sk = *(const u32x4*)(shk + (size_t)(d0 + 2 * cp) * 4);
      f32x2 s0, s1, s2, s3;
      s0.x = bf2f(sk.x & 0xffffu); s1.x = bf2f(sk.x >> 16); s2.x = bf2f(sk.y & 0xffffu); s3.x = bf2f(sk.y >> 16);
      s0.y = bf2f(sk.z & 0xffffu); s1.y = bf2f(sk.z >> 16); s2.y = bf2f(sk.w & 0xffffu); s3.y = bf2f(sk.w >> 16);
      f32x2 K[7];
#pragma unroll
      for (int j = 0; j < 3; ++j) K[j] = unpk(dw(kh[j], cp));
#pragma unroll
      for (int r = 0; r < 4; ++r) K[3 + r] = unpk(dw(kr[r], cp));
      unsigned o[4];
#pragma unroll
      for (int r = 0; r < 4; ++r) {
        f32x2 a = rn2(K[r] * s3);
        a = rn2(a + rn2(K[r + 1] * s2));
        a = rn2(a + rn2(K[r + 2] * s1));
        a = rn2(a + rn2(K[r + 3] * s0));
        o[r] = cvtpk(a * unpk(dw(vr[r], cp)));
      }
      u32x2 lo, hi;
      lo.x = __builtin_amdgcn_perm(o[2], o[3], 0x05040100u);
      lo.y = __builtin_amdgcn_perm(o[0], o[1], 0x05040100u);
      hi.x = __builtin_amdgcn_perm(o[2], o[3], 0x07060302u);
      hi.y = __builtin_amdgcn_perm(o[0], o[1], 0x07060302u);
      *(u32x2*)(smem + (2 * cp) * CV_WAVE + pos) = lo;
      *(u32x2*)(smem + (2 * cp + 1) * CV_WAVE + pos) = hi;
    }
  }
  {
    char* hc0 = smem + w * CV_WAVE + CV_XS;
    char* hc1 = hc0 + CV_HC;
#pragma unroll
    for (int i = 0; i < 4; ++i) {
      unsigned nx = __shfl_down(hv[i].x, 1, 64);
      unsigned nxt_first = (i < 3) ? __shfl(hv[(i < 3) ? i + 1 : 3].x, 0, 64) : 0u;
      if (lane == 63) nx = nxt_first;
      u32x4 sh;
      sh.x = __builtin_amdgcn_alignbyte(hv[i].y, hv[i].x, 2);
      sh.y = __builtin_amdgcn_alignbyte(hv[i].z, hv[i].y, 2);
      sh.z = __builtin_amdgcn_alignbyte(hv[i].w, hv[i].z, 2);
      sh.w = __builtin_amdgcn_alignbyte(nx, hv[i].w, 2);
      *(u32x4*)(hc0 + 32 + (512 * i + 8 * lane) * 2) = hv[i];
      *(u32x4*)(hc1 + 32 + (512 * i + 8 * lane) * 2) = sh;
    }
    unsigned h0 = __shfl(hv[0].x, 0, 64);
    if (lane < 2) *(u32x4*)(hc0 + 16 * lane) = (u32x4){0u, 0u, 0u, 0u};
    if (lane == 2) *(u32x4*)(hc1) = (u32x4){0u, 0u, 0u, 0u};
    if (lane == 3) *(u32x4*)(hc1 + 16) = (u32x4){0u, 0u, 0u, (h0 & 0xffffu) << 16};
    if (lane < 32) *(u32x4*)(smem + w * CV_WAVE + 16 * lane) = (u32x4){0u, 0u, 0u, 0u};
  }
  __syncthreads();
  u32x4 qv[4];
#pragma unroll
  for (int r = 0; r < 4; ++r) qv[r] = *(const u32x4*)(q + (size_t)(r0 + r) * D_DIM + d0);

  const int g = lane >> 4, r16 = lane & 15;
  const char* xs = smem + w * CV_WAVE;
  const char* hc0 = xs + CV_XS;
  const char* abase = (r16 & 1) ? (hc0 + 2 * (25 + r16 - 8 * g)) : (hc0 + CV_HC + 2 * (24 + r16 - 8 * g));
  const char* bbase = xs + 2 * (240 + 16 * r16 + 8 * g);
  f32x4 acc[8];
#pragma unroll
  for (int i = 0; i < 8; ++i) acc[i] = (f32x4){0.f, 0.f, 0.f, 0.f};
  struct CvFrags { bf16x8 af[8], bfm[8]; };
  auto cv_load = [&](CvFrags& f, int rho) {
#pragma unroll
    for (int u = 0; u < 8; ++u) {
      const unsigned* ap = (const unsigned*)(abase + 64 * (8 * u) + 64 * rho);
      u32x4 t;
      t.x = ap[0]; t.y = ap[1]; t.z = ap[2]; t.w = ap[3];
      f.af[u] = __builtin_bit_cast(bf16x8, t);
    }
#pragma unroll
    for (int J = 0; J < 8; ++J) f.bfm[J] = *(const bf16x8*)(bbase + 512 * J - 64 * rho);
  };
  auto cv_mma = [&](const CvFrags& f) {
#pragma unroll
    for (int J = 0; J < 8; ++J)
#pragma unroll
      for (int I = J; I < 8; ++I)
        acc[I] = __builtin_amdgcn_mfma_f32_16x16x32_bf16(f.af[I - J], f.bfm[J], acc[I], 0, 0, 0);
  };
#define ILV24_36() do { _Pragma("unroll") for (int _i = 0; _i < 24; ++_i) { SGB(0x100, 1); SGB(0x8, 1); } SGB(0x8, 12); } while (0)
  {
    CvFrags ca, cb2;
    cv_load(ca, 0);
#pragma unroll 1
    for (int rho = 0; rho < 6; rho += 2) {
      SCHED(); cv_load(cb2, rho + 1); cv_mma(ca); ILV24_36(); SCHED();
      SCHED(); cv_load(ca, rho + 2); cv_mma(cb2); ILV24_36(); SCHED();
    }
    SCHED(); cv_load(cb2, 7); cv_mma(ca); ILV24_36(); SCHED();
    cv_mma(cb2);
  }

  {
    const float Dv = bf2f(ssmD[d0 + w]);
    const f32x2 D2 = {Dv, Dv};
    char* zs = smem + w * CV_WAVE + CV_XS;
#pragma unroll
    for (int I = 0; I < 8; ++I) {
      int n = 256 * I + 16 * r16 + 4 * g;
      u32x2 kvp = *(const u32x2*)(xs + 2 * (256 + (n & ~7) + 4 - (n & 7)));
      f32x2 a01 = {acc[I][0], acc[I][1]}, a23 = {acc[I][2], acc[I][3]};
      u32x2 o;
      o.x = cvtpk(rn2(a01) + rn2(unpk_sw(kvp.y) * D2));
      o.y = cvtpk(rn2(a23) + rn2(unpk_sw(kvp.x) * D2));
      *(u32x2*)(zs + 2 * n) = o;
    }
  }
  __syncthreads();

  {
    u32x2 zc[8];
#pragma unroll
    for (int c = 0; c < 8; ++c) zc[c] = *(const u32x2*)(smem + c * CV_WAVE + CV_XS + 2 * r0);
#pragma unroll
    for (int r = 0; r < 4; ++r) {
      unsigned o[4];
#pragma unroll
      for (int cp = 0; cp < 4; ++cp) {
        unsigned za = (r < 2) ? zc[2 * cp].x : zc[2 * cp].y;
        unsigned zb = (r < 2) ? zc[2 * cp + 1].x : zc[2 * cp + 1].y;
        f32x2 z;
        z.x = __uint_as_float((r & 1) ? (za & 0xffff0000u) : (za << 16));
        z.y = __uint_as_float((r & 1) ? (zb & 0xffff0000u) : (zb << 16));
        o[cp] = cvtpk(unpk(dw(qv[r], cp)) * z);
      }
      u32x4 ov = {o[0], o[1], o[2], o[3]};
      *(u32x4*)(out + (size_t)(r0 + r) * (2 * D_DIM) + d0) = ov;
    }
  }
}

static constexpr int K1_SMEM = G1_NS * G1_STAGE;
static_assert(K1_SMEM >= 8 * CV_WAVE, "lds");

__global__ void __launch_bounds__(512, 2)
k1_kernel(const u16* __restrict__ Cre, const u16* __restrict__ Cim, const u16* __restrict__ x,
          u16* __restrict__ PQ, const u16* __restrict__ q, const u16* __restrict__ k,
          const u16* __restrict__ v, const u16* __restrict__ shk, const u16* __restrict__ ssmk,
          const u16* __restrict__ ssmD, u16* __restrict__ out) {
  __shared__ __attribute__((aligned(16))) char smem[K1_SMEM];
  const int b = blockIdx.x;
  if (b < NGEMM1) big_gemm(smem, b, Cre, Cim, x, PQ);
  else conv_block(smem, b - NGEMM1, q, k, v, shk, ssmk, ssmD, out);
}

static constexpr int G2_NS = 6;
static constexpr int G2_PT = 64 * 128;
static constexpr int G2_WT = 80 * 128;
static constexpr int G2_STAGE = G2_PT + G2_WT;
static constexpr int G2_STEPS = 20;

struct G2Frags {
  bf16x8 pf[2];
  bf16x8 wf[2][5];
};

__global__ void __launch_bounds__(256, 1)
k2_small_gemm(const u16* __restrict__ PQ, const u16* __restrict__ Wre, const u16* __restrict__ Wim,
              u16* __restrict__ out) {
  __shared__ __attribute__((aligned(16))) char smem[G2_NS * G2_STAGE];
  const int b = blockIdx.x;
  const int tid = threadIdx.x;
  const int w = __builtin_amdgcn_readfirstlane(tid >> 6);
  const int lane = tid & 63;
  const int xcd = b & 7, idx = b >> 3;
  const int ttile = xcd * 4 + (idx >> 3);
  const int etile = idx & 7;
  const int t0 = ttile * 64, e0 = etile * 80;
  const unsigned lds0 = __builtin_amdgcn_readfirstlane((unsigned)(size_t)smem);

  unsigned poff[2];
#pragma unroll
  for (int i = 0; i < 2; ++i) {
    int row = 16 * w + 8 * i + (lane >> 3);
    int sc = (lane & 7) ^ ((row >> 1) & 7);
    poff[i] = ((t0 + row) * (2 * D_DIM) + 8 * sc) * 2;
  }
  unsigned woff[3];
#pragma unroll
  for (int i = 0; i < 3; ++i) {
    int ci = 160 * w + 64 * i + lane;
    if (i == 2 && lane >= 32) ci = 160 * w;
    int row = ci >> 3;
    int sc = (ci & 7) ^ ((row >> 1) & 7);
    woff[i] = ((e0 + row) * D_DIM + 8 * sc) * 2;
  }
  auto stage_issue = [&](int step, int buf) {
    unsigned sb = lds0 + buf * G2_STAGE;
    const u16* pg = PQ + step * 64;
    const u16* wg = (step < 10) ? (Wre + step * 64) : (Wim + (step - 10) * 64);
#pragma unroll
    for (int i = 0; i < 2; ++i) glds16(pg, poff[i], sb + (16 * w + 8 * i) * 128);
    glds16(wg, woff[0], sb + G2_PT + (160 * w) * 16);
    glds16(wg, woff[1], sb + G2_PT + (160 * w + 64) * 16);
    if (lane < 32) glds16(wg, woff[2], sb + G2_PT + (160 * w + 128) * 16);
  };
  const int g = lane >> 4, r16 = lane & 15;
  int prd[2], wrd[2];
#pragma unroll
  for (int ks = 0; ks < 2; ++ks) {
    int row = 16 * w + r16;
    int ch = 4 * ks + g;
    prd[ks] = row * 128 + 16 * (ch ^ ((row >> 1) & 7));
    wrd[ks] = G2_PT + r16 * 128 + 16 * (ch ^ ((r16 >> 1) & 7));
  }
  f32x4 acc[5];
#pragma unroll
  for (int i = 0; i < 5; ++i) acc[i] = (f32x4){0.f, 0.f, 0.f, 0.f};
  auto load_frags = [&](G2Frags& f, int buf) {
    const char* sb = smem + buf * G2_STAGE;
#pragma unroll
    for (int ks = 0; ks < 2; ++ks) {
      f.pf[ks] = *(const bf16x8*)(sb + prd[ks]);
#pragma unroll
      for (int et = 0; et < 5; ++et) f.wf[ks][et] = *(const bf16x8*)(sb + wrd[ks] + et * 2048);
    }
  };
  auto mma = [&](const G2Frags& f) {
#pragma unroll
    for (int ks = 0; ks < 2; ++ks)
#pragma unroll
      for (int et = 0; et < 5; ++et)
        acc[et] = __builtin_amdgcn_mfma_f32_16x16x32_bf16(f.wf[ks][et], f.pf[ks], acc[et], 0, 0, 0);
  };
  auto nxt = [](int bb) { return (bb + 1 == G2_NS) ? 0 : bb + 1; };
#pragma unroll
  for (int s = 0; s < G2_NS; ++s) stage_issue(s, s);
  G2Frags fa, fb;
  WAIT_V(25);
  BAR();
  load_frags(fa, 0);
  int bk = 0;
  for (int k = 0; k < G2_STEPS - G2_NS; k += 2) {
    int b1;
    WAIT_V(20); BAR(); stage_issue(k + G2_NS, bk); b1 = nxt(bk); SCHED(); load_frags(fb, b1); mma(fa); ILV12_10(); SCHED(); bk = b1;
    WAIT_V(20); BAR(); stage_issue(k + 1 + G2_NS, bk); b1 = nxt(bk); SCHED(); load_frags(fa, b1); mma(fb); ILV12_10(); SCHED(); bk = b1;
  }
  {
    int b1;
    WAIT_V(20); BAR(); b1 = nxt(bk); SCHED(); load_frags(fb, b1); mma(fa); ILV12_10(); SCHED(); bk = b1;
    WAIT_V(15); BAR(); b1 = nxt(bk); SCHED(); load_frags(fa, b1); mma(fb); ILV12_10(); SCHED(); bk = b1;
    WAIT_V(10); BAR(); b1 = nxt(bk); SCHED(); load_frags(fb, b1); mma(fa); ILV12_10(); SCHED(); bk = b1;
    WAIT_V(5);  BAR(); b1 = nxt(bk); SCHED(); load_frags(fa, b1); mma(fb); ILV12_10(); SCHED(); bk = b1;
    WAIT_V(0);  BAR(); b1 = nxt(bk); SCHED(); load_frags(fb, b1); mma(fa); ILV12_10(); SCHED();
    mma(fb);
  }

  u16* op = out + (size_t)(t0 + 16 * w + r16) * (2 * D_DIM) + D_DIM + e0 + 4 * g;
#pragma unroll
  for (int et = 0; et < 5; ++et) {
    u32x2 v;
    v.x = pack2(acc[et][0], acc[et][1]);
    v.y = pack2(acc[et][2], acc[et][3]);
    *(u32x2*)(op + 16 * et) = v;
  }
}

extern "C" void kernel_launch(void* const* d_in, const int* in_sizes, int n_in, void* d_out,
                              int out_size, void* d_ws, size_t ws_size, hipStream_t stream) {
  const u16* q = (const u16*)d_in[0];
  const u16* k = (const u16*)d_in[1];
  const u16* v = (const u16*)d_in[2];
  const u16* shk = (const u16*)d_in[3];
  const u16* ssmk = (const u16*)d_in[4];
  const u16* ssmD = (const u16*)d_in[5];
  const u16* x = (const u16*)d_in[6];
  const u16* Cre = (const u16*)d_in[7];
  const u16* Cim = (const u16*)d_in[8];
  const u16* Wre = (const u16*)d_in[9];
  const u16* Wim = (const u16*)d_in[10];
  u16* out = (u16*)d_out;
  u16* PQ = (u16*)d_ws;

  k1_kernel<<<NGEMM1 + NCONV, 512, 0, stream>>>(Cre, Cim, x, PQ, q, k, v, shk, ssmk, ssmD, out);
  k2_small_gemm<<<256, 256, 0, stream>>>(PQ, Wre, Wim, out);
}
```

```cpp
#include <hip/hip_runtime.h>
#include <hip/hip_bf16.h>
#include <stdint.h>

typedef __attribute__((ext_vector_type(8))) short bf16x8;
typedef __attribute__((ext_vector_type(4))) short s16x4;
typedef __attribute__((ext_vector_type(4))) float f32x4;
typedef __attribute__((ext_vector_type(2))) float f32x2;
typedef __attribute__((ext_vector_type(2))) __bf16 bf16x2v;
typedef __attribute__((ext_vector_type(4))) unsigned u32x4;
typedef __attribute__((ext_vector_type(2))) unsigned u32x2;
typedef unsigned short u16;

#define LDS_AS __attribute__((address_space(3)))

static constexpr int S_LEN = 2048;
static constexpr int D_DIM = 640;

__device__ __forceinline__ float bf2f(unsigned h) { return __uint_as_float(h << 16); }
__device__ __forceinline__ unsigned cvtpk(f32x2 v) {
  bf16x2v b = __builtin_convertvector(v, bf16x2v);
  return __builtin_bit_cast(unsigned, b);
}
__device__ __forceinline__ unsigned pack2(float lo, float hi) { return cvtpk((f32x2){lo, hi}); }
__device__ __forceinline__ f32x2 unpk(unsigned u) {
  f32x2 r;
  r.x = __uint_as_float(u << 16);
  r.y = __uint_as_float(u & 0xffff0000u);
  return r;
}
__device__ __forceinline__ f32x2 unpk_sw(unsigned u) {
  f32x2 r;
  r.x = __uint_as_float(u & 0xffff0000u);
  r.y = __uint_as_float(u << 16);
  return r;
}
__device__ __forceinline__ f32x2 rn2(f32x2 v) { return unpk(cvtpk(v)); }
__device__ __forceinline__ unsigned dw(const u32x4& v, int i) {
  return (i == 0) ? v.x : (i == 1) ? v.y : (i == 2) ? v.z : v.w;
}

#define WAIT_V(n) asm volatile("s_waitcnt vmcnt(" #n ") lgkmcnt(0)" ::: "memory")
#define WAIT_L() asm volatile("s_waitcnt lgkmcnt(0)" ::: "memory")
#define BAR() __builtin_amdgcn_s_barrier()
#define SCHED() __builtin_amdgcn_sched_barrier(0)
#define SGB(mask, n) __builtin_amdgcn_sched_group_barrier(mask, n, 0)

__device__ __forceinline__ void glds16(const void* sbase, unsigned voff, unsigned lds_dst) {
  unsigned keep;
  asm volatile(
      "s_mov_b32 %0, m0\n\ts_mov_b32 m0, %3\n\ts_nop 0\n\tglobal_load_lds_dwordx4 %1, %2\n\ts_mov_b32 m0, %0"
      : "=&s"(keep)
      : "v"(voff), "s"(sbase), "s"(lds_dst)
      : "memory");
}
__device__ __forceinline__ void touch(const void* p) {
  unsigned t;
  asm volatile("global_load_dword %0, %1, off\n\ts_waitcnt vmcnt(0)" : "=v"(t) : "v"(p) : "memory");
}

static constexpr int G1_NS = 5;
static constexpr int G1_CT = 128 * 128;
static constexpr int G1_XT = 64 * 256;
static constexpr int G1_STAGE = G1_CT + G1_XT;
static constexpr int NGEMM1 = 160;
static constexpr int NHELP = 96;

struct G1Slice {
  bf16x8 cf[4];
  bf16x8 xf[4];
};

__device__ __forceinline__ void big_gemm(char* smem, int b, const u16* __restrict__ Cre,
                                         const u16* __restrict__ Cim, const u16* __restrict__ x,
                                         u16* __restrict__ PQ) {
  const int tid = threadIdx.x;
  const int w = __builtin_amdgcn_readfirstlane(tid >> 6);
  const int lane = tid & 63;
  const int xcd = b & 7, idx = b >> 3;
  const int ttile = xcd * 4 + idx / 5;
  const int dtile = idx % 5;
  const int t0 = ttile * 128;
  const int is_im = t0 >= S_LEN;
  const int trow0 = t0 & (S_LEN - 1);
  const int d0 = dtile * 128;
  auto nxt = [](int bb) { return (bb + 1 == G1_NS) ? 0 : bb + 1; };
  const int g = lane >> 4, r16 = lane & 15;

  if (w >= 4) {
    const int lw = w - 4;
    const u16* Cp = (is_im ? Cim : Cre) + (size_t)trow0 * S_LEN;
    const u16* xp = x + d0;
    const unsigned lds0 = __builtin_amdgcn_readfirstlane((unsigned)(size_t)smem);
    unsigned coff[2], xoff[2];
#pragma unroll
    for (int i = 0; i < 2; ++i) {
      int row = 16 * lw + 8 * i + (lane >> 3);
      int sc = (lane & 7) ^ ((row >> 1) & 7);
      coff[i] = (row * S_LEN + 8 * sc) * 2;
      int xr = 8 * lw + 4 * i + (lane >> 4);
      int r3 = (((xr >> 3) & 1) << 2) | (xr & 3);
      int xc = (lane & 15) ^ (r3 << 1);
      xoff[i] = (xr * D_DIM + 8 * xc) * 2;
    }
    auto stage_issue = [&](int step, int buf) {
      unsigned sb = lds0 + buf * G1_STAGE;
      const u16* cg = Cp + step * 64;
      const u16* xg = xp + (size_t)step * 64 * D_DIM;
#pragma unroll
      for (int i = 0; i < 2; ++i) {
        glds16(cg, coff[i], sb + (16 * lw + 8 * i) * 128);
        glds16(xg, xoff[i], sb + G1_CT + (8 * lw + 4 * i) * 256);
      }
    };
#pragma unroll
    for (int s = 0; s < G1_NS; ++s) stage_issue(s, s);
    WAIT_V(16);
    BAR();
    int bk = 0;
    for (int k = 0; k < 27; ++k) {
      WAIT_V(12);
      BAR();
      stage_issue(k + G1_NS, bk);
      bk = nxt(bk);
    }
    WAIT_V(12); BAR();
    WAIT_V(8);  BAR();
    WAIT_V(4);  BAR();
    WAIT_V(0);  BAR();
  } else {
    const int wt = w >> 1, wd = w & 1;
    int crd[2];
#pragma unroll
    for (int ks = 0; ks < 2; ++ks)
      crd[ks] = (64 * wt + r16) * 128 + 16 * ((4 * ks + g) ^ ((r16 >> 1) & 7));
    const int q4 = (lane & 15) >> 2, p4 = lane & 3;
    int xrd[4];
#pragma unroll
    for (int dt = 0; dt < 4; ++dt)
      xrd[dt] = G1_CT + (8 * g + q4) * 256 + 32 * ((4 * wd + dt) ^ (((g & 1) << 2) | q4)) + 8 * p4;

    f32x4 acc[4][4];
#pragma unroll
    for (int i = 0; i < 4; ++i)
#pragma unroll
      for (int j = 0; j < 4; ++j) acc[i][j] = (f32x4){0.f, 0.f, 0.f, 0.f};

    auto load_slice = [&](G1Slice& f, int buf, int ks) {
      const char* sb = smem + buf * G1_STAGE;
#pragma unroll
      for (int dt = 0; dt < 4; ++dt) {
        s16x4 lo = __builtin_amdgcn_ds_read_tr16_b64_v4i16((LDS_AS s16x4*)(sb + xrd[dt] + (32 * ks) * 256));
        s16x4 hi = __builtin_amdgcn_ds_read_tr16_b64_v4i16((LDS_AS s16x4*)(sb + xrd[dt] + (32 * ks + 4) * 256));
        f.xf[dt] = __builtin_shufflevector(lo, hi, 0, 1, 2, 3, 4, 5, 6, 7);
      }
#pragma unroll
      for (int tt = 0; tt < 4; ++tt) f.cf[tt] = *(const bf16x8*)(sb + crd[ks] + tt * 2048);
    };
    auto mma = [&](const G1Slice& f) {
#pragma unroll
      for (int dt = 0; dt < 4; ++dt)
#pragma unroll
        for (int tt = 0; tt < 4; ++tt)
          acc[dt][tt] = __builtin_amdgcn_mfma_f32_16x16x32_bf16(f.xf[dt], f.cf[tt], acc[dt][tt], 0, 0, 0);
    };
#define ILV12_16() do { _Pragma("unroll") for (int _i = 0; _i < 12; ++_i) { SGB(0x100, 1); SGB(0x8, 1); } SGB(0x8, 4); } while (0)
    G1Slice f0, f1;
    BAR();
    load_slice(f0, 0, 0);
    int bk = 0;
    for (int k = 0; k < 31; ++k) {
      SCHED(); load_slice(f1, bk, 1); mma(f0); ILV12_16(); SCHED();
      WAIT_L();
      BAR();
      bk = nxt(bk);
      SCHED(); load_slice(f0, bk, 0); mma(f1); ILV12_16(); SCHED();
    }
    SCHED(); load_slice(f1, bk, 1); mma(f0); ILV12_16(); SCHED();
    mma(f1);
    WAIT_L();
    const float sgn = is_im ? -1.f : 1.f;
    char* ob = smem + 3 * G1_STAGE;
#pragma unroll
    for (int tt = 0; tt < 4; ++tt)
#pragma unroll
      for (int dt = 0; dt < 4; ++dt) {
        u32x2 v;
        v.x = pack2(sgn * acc[dt][tt][0], sgn * acc[dt][tt][1]);
        v.y = pack2(sgn * acc[dt][tt][2], sgn * acc[dt][tt][3]);
        *(u32x2*)(ob + (64 * wt + 16 * tt + r16) * 272 + (64 * wd + 16 * dt + 4 * g) * 2) = v;
      }
  }
  __syncthreads();
  {
    const char* ob = smem + 3 * G1_STAGE;
    u16* outp = PQ + (size_t)trow0 * (2 * D_DIM) + (is_im ? D_DIM : 0) + d0;
#pragma unroll
    for (int i = 0; i < 3; ++i) {
      int row = 4 * (w + 12 * i) + g;
      if (row < 128) {
        u32x4 v = *(const u32x4*)(ob + row * 272 + r16 * 16);
        *(u32x4*)(outp + (size_t)row * (2 * D_DIM) + r16 * 8) = v;
      }
    }
  }
}

__device__ __forceinline__ void helper_block(int hb, const u16* __restrict__ Cre, const u16* __restrict__ Cim,
                                             const u16* __restrict__ x, const u16* __restrict__ q,
                                             const u16* __restrict__ k, const u16* __restrict__ v,
                                             const u16* __restrict__ shk, const u16* __restrict__ ssmk,
                                             const u16* __restrict__ Wre, const u16* __restrict__ Wim,
                                             u16* __restrict__ kvT) {
  const int w = __builtin_amdgcn_readfirstlane(threadIdx.x >> 6);
  const int lane = threadIdx.x & 63;
  const int xcd = hb & 7;
  const int gw = hb * 4 + w;
  if (w >= 4) {
    const int wi = (hb >> 3) * 8 + (w - 4);
    constexpr int NW = NHELP / 8 * 8;
#pragma unroll 1
    for (int u = wi; u < 32 * 18; u += NW) {
      const int step = u / 18, n = u - step * 18;
      const char* p;
      if (n < 8) {
        int row = 512 * xcd + 64 * n + lane;
        const u16* cb = (row >= S_LEN) ? Cim : Cre;
        p = (const char*)(cb + (size_t)(row & (S_LEN - 1)) * S_LEN) + step * 128;
      } else {
        p = (const char*)x + (size_t)step * (64 * D_DIM * 2) + (n - 8) * 8192 + lane * 128;
      }
      touch(p);
    }
#pragma unroll 1
    for (int u = hb * 8 + (w - 4); u < 840; u += NHELP * 8) {
      const char* p;
      if (u < 320) p = (const char*)q + (size_t)u * 8192;
      else if (u < 640) p = (const char*)ssmk + (size_t)(u - 320) * 8192;
      else if (u < 740) p = (const char*)Wre + (size_t)(u - 640) * 8192;
      else p = (const char*)Wim + (size_t)(u - 740) * 8192;
      touch(p + lane * 128);
    }
  } else if (gw < 320) {
    const int s0 = (gw / 10) * 64, c0 = (gw % 10) * 64;
    const int rg = lane >> 3, cc = lane & 7;
    const int d = c0 + 8 * cc;
    const int r0 = s0 + 8 * rg;
    u32x4 kr[8], vr[8], kh[3];
#pragma unroll
    for (int r = 0; r < 8; ++r) kr[r] = *(const u32x4*)(k + (size_t)(r0 + r) * D_DIM + d);
#pragma unroll
    for (int j = 0; j < 3; ++j) {
      if (s0 == 0) kh[j] = (u32x4){0u, 0u, 0u, 0u};
      else kh[j] = *(const u32x4*)(k + (size_t)(s0 - 3 + j) * D_DIM + d);
    }
#pragma unroll
    for (int r = 0; r < 8; ++r) vr[r] = *(const u32x4*)(v + (size_t)(r0 + r) * D_DIM + d);
    u32x4 sk[4];
#pragma unroll
    for (int cp = 0; cp < 4; ++cp) sk[cp] = *(const u32x4*)(shk + (size_t)(d + 2 * cp) * 4);
#pragma unroll
    for (int j = 0; j < 3; ++j) {
      u32x4 t;
      t.x = __shfl_up(kr[5 + j].x, 8, 64);
      t.y = __shfl_up(kr[5 + j].y, 8, 64);
      t.z = __shfl_up(kr[5 + j].z, 8, 64);
      t.w = __shfl_up(kr[5 + j].w, 8, 64);
      if (rg != 0) kh[j] = t;
    }
#pragma unroll
    for (int cp = 0; cp < 4; ++cp) {
      f32x2 s0v, s1v, s2v, s3v;
      s0v.x = bf2f(sk[cp].x & 0xffffu); s1v.x = bf2f(sk[cp].x >> 16); s2v.x = bf2f(sk[cp].y & 0xffffu); s3v.x = bf2f(sk[cp].y >> 16);
      s0v.y = bf2f(sk[cp].z & 0xffffu); s1v.y = bf2f(sk[cp].z >> 16); s2v.y = bf2f(sk[cp].w & 0xffffu); s3v.y = bf2f(sk[cp].w >> 16);
      f32x2 K[11];
#pragma unroll
      for (int j = 0; j < 3; ++j) K[j] = unpk(dw(kh[j], cp));
#pragma unroll
      for (int r = 0; r < 8; ++r) K[3 + r] = unpk(dw(kr[r], cp));
      unsigned o[8];
#pragma unroll
      for (int r = 0; r < 8; ++r) {
        f32x2 a = rn2(K[r] * s3v);
        a = rn2(a + rn2(K[r + 1] * s2v));
        a = rn2(a + rn2(K[r + 2] * s1v));
        a = rn2(a + rn2(K[r + 3] * s0v));
        o[r] = cvtpk(a * unpk(dw(vr[r], cp)));
      }
      u32x4 lo, hi;
      lo.x = __builtin_amdgcn_perm(o[6], o[7], 0x05040100u); hi.x = __builtin_amdgcn_perm(o[6], o[7], 0x07060302u);
      lo.y = __builtin_amdgcn_perm(o[4], o[5], 0x05040100u); hi.y = __builtin_amdgcn_perm(o[4], o[5], 0x07060302u);
      lo.z = __builtin_amdgcn_perm(o[2], o[3], 0x05040100u); hi.z = __builtin_amdgcn_perm(o[2], o[3], 0x07060302u);
      lo.w = __builtin_amdgcn_perm(o[0], o[1], 0x05040100u); hi.w = __builtin_amdgcn_perm(o[0], o[1], 0x07060302u);
      *(u32x4*)(kvT + (size_t)(d + 2 * cp) * S_LEN + r0) = lo;
      *(u32x4*)(kvT + (size_t)(d + 2 * cp + 1) * S_LEN + r0) = hi;
    }
  }
}

static constexpr int K1_SMEM = G1_NS * G1_STAGE;

__global__ void __launch_bounds__(768, 3)
k1_kernel(const u16* __restrict__ Cre, const u16* __restrict__ Cim, const u16* __restrict__ x,
          u16* __restrict__ PQ, const u16* __restrict__ q, const u16* __restrict__ k,
          const u16* __restrict__ v, const u16* __restrict__ shk, const u16* __restrict__ ssmk,
          const u16* __restrict__ Wre, const u16* __restrict__ Wim, u16* __restrict__ kvT) {
  __shared__ __attribute__((aligned(16))) char smem[K1_SMEM];
  const int b = blockIdx.x;
  if (b < NGEMM1) big_gemm(smem, b, Cre, Cim, x, PQ);
  else helper_block(b - NGEMM1, Cre, Cim, x, q, k, v, shk, ssmk, Wre, Wim, kvT);
}

static constexpr int NCONV = 80;
static constexpr int CV_XS = (256 + 2048) * 2;
static constexpr int CV_HC = 2080 * 2;
static constexpr int CV_WAVE = CV_XS + 2 * CV_HC;

__device__ __forceinline__ void conv_block(char* smem, int cb, const u16* __restrict__ q,
                                           const u16* __restrict__ kvT, const u16* __restrict__ ssmk,
                                           const u16* __restrict__ ssmD, u16* __restrict__ out) {
  const int tid = threadIdx.x;
  const int w = __builtin_amdgcn_readfirstlane(tid >> 6);
  const int lane = tid & 63;
  const int grp = (cb & 7) * 10 + (cb >> 3);
  const int d0 = grp * 8;
  const int r0 = 4 * tid;

  const float Dv = bf2f(ssmD[d0 + w]);
  char* xs = smem + w * CV_WAVE;
  char* hc0 = xs + CV_XS;
  {
    u32x4 xv[4], hv[4];
    const u16* xg = kvT + (size_t)(d0 + w) * S_LEN;
    const u16* hg = ssmk + (size_t)(d0 + w) * S_LEN;
#pragma unroll
    for (int i = 0; i < 4; ++i) {
      xv[i] = *(const u32x4*)(xg + 512 * i + 8 * lane);
      hv[i] = *(const u32x4*)(hg + 512 * i + 8 * lane);
    }
    char* hc1 = hc0 + CV_HC;
#pragma unroll
    for (int i = 0; i < 4; ++i) {
      *(u32x4*)(xs + (256 + 512 * i + 8 * lane) * 2) = xv[i];
      unsigned nx = __shfl_down(hv[i].x, 1, 64);
      unsigned nxt_first = (i < 3) ? __shfl(hv[(i < 3) ? i + 1 : 3].x, 0, 64) : 0u;
      if (lane == 63) nx = nxt_first;
      u32x4 sh;
      sh.x = __builtin_amdgcn_alignbyte(hv[i].y, hv[i].x, 2);
      sh.y = __builtin_amdgcn_alignbyte(hv[i].z, hv[i].y, 2);
      sh.z = __builtin_amdgcn_alignbyte(hv[i].w, hv[i].z, 2);
      sh.w = __builtin_amdgcn_alignbyte(nx, hv[i].w, 2);
      *(u32x4*)(hc0 + 32 + (512 * i + 8 * lane) * 2) = hv[i];
      *(u32x4*)(hc1 + 32 + (512 * i + 8 * lane) * 2) = sh;
    }
    unsigned h0 = __shfl(hv[0].x, 0, 64);
    if (lane < 2) *(u32x4*)(hc0 + 16 * lane) = (u32x4){0u, 0u, 0u, 0u};
    if (lane == 2) *(u32x4*)(hc1) = (u32x4){0u, 0u, 0u, 0u};
    if (lane == 3) *(u32x4*)(hc1 + 16) = (u32x4){0u, 0u, 0u, (h0 & 0xffffu) << 16};
    if (lane < 32) *(u32x4*)(xs + 16 * lane) = (u32x4){0u, 0u, 0u, 0u};
  }
  WAIT_L();
  u32x4 qv[4];
  auto qload = [&](int i) { qv[i] = *(const u32x4*)(q + (size_t)(r0 + i) * D_DIM + d0); };

  const int g = lane >> 4, r16 = lane & 15;
  const char* abase = (r16 & 1) ? (hc0 + 2 * (25 + r16 - 8 * g)) : (hc0 + CV_HC + 2 * (24 + r16 - 8 * g));
  const char* bbase = xs + 2 * (240 + 16 * r16 + 8 * g);
  f32x4 acc[8];
#pragma unroll
  for (int i = 0; i < 8; ++i) acc[i] = (f32x4){0.f, 0.f, 0.f, 0.f};
  struct CvFrags { bf16x8 af[8], bfm[8]; };
  auto cv_load = [&](CvFrags& f, int rho) {
#pragma unroll
    for (int u = 0; u < 8; ++u) {
      const unsigned* ap = (const unsigned*)(abase + 64 * (8 * u) + 64 * rho);
      u32x4 t;
      t.x = ap[0]; t.y = ap[1]; t.z = ap[2]; t.w = ap[3];
      f.af[u] = __builtin_bit_cast(bf16x8, t);
    }
#pragma unroll
    for (int J = 0; J < 8; ++J) f.bfm[J] = *(const bf16x8*)(bbase + 512 * J - 64 * rho);
  };
  auto cv_mma = [&](const CvFrags& f) {
#pragma unroll
    for (int J = 0; J < 8; ++J)
#pragma unroll
      for (int I = J; I < 8; ++I)
        acc[I] = __builtin_amdgcn_mfma_f32_16x16x32_bf16(f.af[I - J], f.bfm[J], acc[I], 0, 0, 0);
  };
#define ILV24_36() do { _Pragma("unroll") for (int _i = 0; _i < 24; ++_i) { SGB(0x100, 1); SGB(0x8, 1); } SGB(0x8, 12); } while (0)
  {
    CvFrags ca, cb2;
    cv_load(ca, 0);
#pragma unroll
    for (int rho = 0; rho < 6; rho += 2) {
      qload(rho >> 1);
      SCHED(); cv_load(cb2, rho + 1); cv_mma(ca); ILV24_36(); SCHED();
      SCHED(); cv_load(ca, rho + 2); cv_mma(cb2); ILV24_36(); SCHED();
    }
    qload(3);
    SCHED(); cv_load(cb2, 7); cv_mma(ca); ILV24_36(); SCHED();
    cv_mma(cb2);
  }

  {
    const f32x2 D2 = {Dv, Dv};
#pragma unroll
    for (int I = 0; I < 8; ++I) {
      int n = 256 * I + 16 * r16 + 4 * g;
      u32x2 kvp = *(const u32x2*)(xs + 2 * (256 + (n & ~7) + 4 - (n & 7)));
      f32x2 a01 = {acc[I][0], acc[I][1]}, a23 = {acc[I][2], acc[I][3]};
      u32x2 o;
      o.x = cvtpk(rn2(a01) + rn2(unpk_sw(kvp.y) * D2));
      o.y = cvtpk(rn2(a23) + rn2(unpk_sw(kvp.x) * D2));
      *(u32x2*)(hc0 + 2 * n) = o;
    }
  }
  __syncthreads();

  {
    u32x2 zc[8];
#pragma unroll
    for (int c = 0; c < 8; ++c) zc[c] = *(const u32x2*)(smem + c * CV_WAVE + CV_XS + 2 * r0);
#pragma unroll
    for (int r = 0; r < 4; ++r) {
      unsigned o[4];
#pragma unroll
      for (int cp = 0; cp < 4; ++cp) {
        unsigned za = (r < 2) ? zc[2 * cp].x : zc[2 * cp].y;
        unsigned zb = (r < 2) ? zc[2 * cp + 1].x : zc[2 * cp + 1].y;
        f32x2 z;
        z.x = __uint_as_float((r & 1) ? (za & 0xffff0000u) : (za << 16));
        z.y = __uint_as_float((r & 1) ? (zb & 0xffff0000u) : (zb << 16));
        o[cp] = cvtpk(unpk(dw(qv[r], cp)) * z);
      }
      u32x4 ov = {o[0], o[1], o[2], o[3]};
      *(u32x4*)(out + (size_t)(r0 + r) * (2 * D_DIM) + d0) = ov;
    }
  }
}

static constexpr int G2_NS = 6;
static constexpr int G2_PT = 128 * 128;
static constexpr int G2_WT = 80 * 128;
static constexpr int G2_STAGE = G2_PT + G2_WT;
static constexpr int NGEMM2 = 128;

struct G2Slice {
  bf16x8 pf[2];
  bf16x8 wf[5];
};

__device__ __forceinline__ void small_gemm(char* smem, int b, const u16* __restrict__ PQ,
                                           const u16* __restrict__ Wre, const u16* __restrict__ Wim,
                                           u16* __restrict__ out) {
  const int tid = threadIdx.x;
  const int w = __builtin_amdgcn_readfirstlane(tid >> 6);
  const int lane = tid & 63;
  const int xcd = b & 7, idx = b >> 3;
  const int ttile = xcd * 2 + (idx >> 3);
  const int etile = idx & 7;
  const int t0 = ttile * 128, e0 = etile * 80;
  auto nxt = [](int bb) { return (bb + 1 == G2_NS) ? 0 : bb + 1; };
  const int g = lane >> 4, r16 = lane & 15;

  if (w >= 4) {
    const int lw = w - 4;
    const unsigned lds0 = __builtin_amdgcn_readfirstlane((unsigned)(size_t)smem);
    unsigned poff[4], woff[3];
#pragma unroll
    for (int i = 0; i < 4; ++i) {
      int row = 32 * lw + 8 * i + (lane >> 3);
      int sc = (lane & 7) ^ ((row >> 1) & 7);
      poff[i] = ((t0 + row) * (2 * D_DIM) + 8 * sc) * 2;
    }
#pragma unroll
    for (int i = 0; i < 3; ++i) {
      int ci = 160 * lw + 64 * i + lane;
      if (i == 2 && lane >= 32) ci = 160 * lw;
      int wr = ci >> 3;
      int wc = (ci & 7) ^ ((wr >> 1) & 7);
      woff[i] = ((e0 + wr) * D_DIM + 8 * wc) * 2;
    }
    auto stage_issue = [&](int step, int buf) {
      unsigned sb = lds0 + buf * G2_STAGE;
      const u16* pg = PQ + step * 64;
      const u16* wg = (step < 10) ? (Wre + step * 64) : (Wim + (step - 10) * 64);
#pragma unroll
      for (int i = 0; i < 4; ++i) glds16(pg, poff[i], sb + (32 * lw + 8 * i) * 128);
      glds16(wg, woff[0], sb + G2_PT + (160 * lw) * 16);
      glds16(wg, woff[1], sb + G2_PT + (160 * lw + 64) * 16);
      if (lane < 32) glds16(wg, woff[2], sb + G2_PT + (160 * lw + 128) * 16);
    };
    stage_issue(0, 0);
    stage_issue(1, 1);
    WAIT_V(7);
    BAR();
#pragma unroll
    for (int s = 2; s < G2_NS; ++s) stage_issue(s, s);
    int bk = 0;
    for (int k = 0; k < 14; ++k) {
      WAIT_V(28);
      BAR();
      stage_issue(k + G2_NS, bk);
      bk = nxt(bk);
    }
    WAIT_V(28); BAR();
    WAIT_V(21); BAR();
    WAIT_V(14); BAR();
    WAIT_V(7);  BAR();
    WAIT_V(0);  BAR();
  } else {
    int prd[2], wrd[2];
#pragma unroll
    for (int ks = 0; ks < 2; ++ks) {
      int ch = 4 * ks + g;
      prd[ks] = (32 * w + r16) * 128 + 16 * (ch ^ ((r16 >> 1) & 7));
      wrd[ks] = G2_PT + r16 * 128 + 16 * (ch ^ ((r16 >> 1) & 7));
    }
    f32x4 acc[5][2];
#pragma unroll
    for (int i = 0; i < 5; ++i) { acc[i][0] = (f32x4){0.f, 0.f, 0.f, 0.f}; acc[i][1] = (f32x4){0.f, 0.f, 0.f, 0.f}; }
    auto load_slice = [&](G2Slice& f, int buf, int ks) {
      const char* sb = smem + buf * G2_STAGE;
      f.pf[0] = *(const bf16x8*)(sb + prd[ks]);
      f.pf[1] = *(const bf16x8*)(sb + prd[ks] + 2048);
#pragma unroll
      for (int et = 0; et < 5; ++et) f.wf[et] = *(const bf16x8*)(sb + wrd[ks] + et * 2048);
    };
    auto mma = [&](const G2Slice& f) {
#pragma unroll
      for (int et = 0; et < 5; ++et)
#pragma unroll
        for (int tt = 0; tt < 2; ++tt)
          acc[et][tt] = __builtin_amdgcn_mfma_f32_16x16x32_bf16(f.wf[et], f.pf[tt], acc[et][tt], 0, 0, 0);
    };
#define ILV7_10() do { _Pragma("unroll") for (int _i = 0; _i < 7; ++_i) { SGB(0x100, 1); SGB(0x8, 1); } SGB(0x8, 3); } while (0)
    G2Slice f0, f1;
    BAR();
    load_slice(f0, 0, 0);
    int bk = 0;
    for (int k = 0; k < 19; ++k) {
      SCHED(); load_slice(f1, bk, 1); mma(f0); ILV7_10(); SCHED();
      WAIT_L();
      BAR();
      bk = nxt(bk);
      SCHED(); load_slice(f0, bk, 0); mma(f1); ILV7_10(); SCHED();
    }
    SCHED(); load_slice(f1, bk, 1); mma(f0); ILV7_10(); SCHED();
    mma(f1);
    WAIT_L();
    char* ob = smem + 3 * G2_STAGE;
#pragma unroll
    for (int et = 0; et < 5; ++et)
#pragma unroll
      for (int tt = 0; tt < 2; ++tt) {
        u32x2 v;
        v.x = pack2(acc[et][tt][0], acc[et][tt][1]);
        v.y = pack2(acc[et][tt][2], acc[et][tt][3]);
        *(u32x2*)(ob + (32 * w + 16 * tt + r16) * 176 + (16 * et + 4 * g) * 2) = v;
      }
  }
  __syncthreads();
  {
    const char* ob = smem + 3 * G2_STAGE;
#pragma unroll
    for (int i = 0; i < 3; ++i) {
      int c = tid + 512 * i;
      if (c < 1280) {
        int row = c / 10, cc = c - row * 10;
        u32x4 v = *(const u32x4*)(ob + row * 176 + cc * 16);
        *(u32x4*)(out + (size_t)(t0 + row) * (2 * D_DIM) + D_DIM + e0 + cc * 8) = v;
      }
    }
  }
}

static constexpr int NHELP2 = 48;
__device__ __forceinline__ void helper2_block(int hb, const u16* __restrict__ PQ, const u16* __restrict__ Wre,
                                              const u16* __restrict__ Wim) {
  const int w = __builtin_amdgcn_readfirstlane(threadIdx.x >> 6);
  const int lane = threadIdx.x & 63;
  const int xcd = hb & 7;
  const int wi = (hb >> 3) * 8 + w;
  constexpr int NW = NHELP2 / 8 * 8;
#pragma unroll 1
  for (int u = wi; u < 20 * 14; u += NW) {
    const int step = u / 14, n = u - step * 14;
    const char* p;
    if (n < 4) p = (const char*)(PQ + (size_t)(256 * xcd + 64 * n + lane) * (2 * D_DIM)) + step * 128;
    else {
      const u16* wb = (step < 10) ? Wre : Wim;
      p = (const char*)(wb + (size_t)(64 * (n - 4) + lane) * D_DIM) + (step < 10 ? step : step - 10) * 128;
    }
    touch(p);
  }
}

static constexpr int K2_SMEM = G2_NS * G2_STAGE;
static_assert(K2_SMEM >= 8 * CV_WAVE, "lds");

__global__ void __launch_bounds__(512, 2)
k2_kernel(const u16* __restrict__ PQ, const u16* __restrict__ Wre, const u16* __restrict__ Wim,
          const u16* __restrict__ q, const u16* __restrict__ kvT, const u16* __restrict__ ssmk,
          const u16* __restrict__ ssmD, u16* __restrict__ out) {
  __shared__ __attribute__((aligned(16))) char smem[K2_SMEM];
  const int b = blockIdx.x;
  if (b < NCONV) conv_block(smem, b, q, kvT, ssmk, ssmD, out);
  else if (b < NCONV + NGEMM2) small_gemm(smem, b - NCONV, PQ, Wre, Wim, out);
  else helper2_block(b - NCONV - NGEMM2, PQ, Wre, Wim);
}

extern "C" void kernel_launch(void* const* d_in, const int* in_sizes, int n_in, void* d_out,
                              int out_size, void* d_ws, size_t ws_size, hipStream_t stream) {
  const u16* q = (const u16*)d_in[0];
  const u16* k = (const u16*)d_in[1];
  const u16* v = (const u16*)d_in[2];
  const u16* shk = (const u16*)d_in[3];
  const u16* ssmk = (const u16*)d_in[4];
  const u16* ssmD = (const u16*)d_in[5];
  const u16* x = (const u16*)d_in[6];
  const u16* Cre = (const u16*)d_in[7];
  const u16* Cim = (const u16*)d_in[8];
  const u16* Wre = (const u16*)d_in[9];
  const u16* Wim = (const u16*)d_in[10];
  u16* out = (u16*)d_out;
  u16* PQ = (u16*)d_ws;
  u16* kvT = PQ + (size_t)S_LEN * 2 * D_DIM;

  k1_kernel<<<NGEMM1 + NHELP, 768, 0, stream>>>(Cre, Cim, x, PQ, q, k, v, shk, ssmk, Wre, Wim, kvT);
  k2_kernel<<<NCONV + NGEMM2 + NHELP2, 512, 0, stream>>>(PQ, Wre, Wim, q, kvT, ssmk, ssmD, out);
}
```

```cpp
#include <hip/hip_runtime.h>
#include <hip/hip_bf16.h>
#include <stdint.h>

typedef __attribute__((ext_vector_type(8))) short bf16x8;
typedef __attribute__((ext_vector_type(4))) short s16x4;
typedef __attribute__((ext_vector_type(4))) float f32x4;
typedef __attribute__((ext_vector_type(16))) float f32x16;
typedef __attribute__((ext_vector_type(2))) float f32x2;
typedef __attribute__((ext_vector_type(2))) __bf16 bf16x2v;
typedef __attribute__((ext_vector_type(4))) unsigned u32x4;
typedef __attribute__((ext_vector_type(2))) unsigned u32x2;
typedef unsigned short u16;

#define LDS_AS __attribute__((address_space(3)))

static constexpr int S_LEN = 2048;
static constexpr int D_DIM = 640;

__device__ __forceinline__ float bf2f(unsigned h) { return __uint_as_float(h << 16); }
__device__ __forceinline__ unsigned cvtpk(f32x2 v) {
  bf16x2v b = __builtin_convertvector(v, bf16x2v);
  return __builtin_bit_cast(unsigned, b);
}
__device__ __forceinline__ unsigned pack2(float lo, float hi) { return cvtpk((f32x2){lo, hi}); }
__device__ __forceinline__ f32x2 unpk(unsigned u) {
  f32x2 r;
  r.x = __uint_as_float(u << 16);
  r.y = __uint_as_float(u & 0xffff0000u);
  return r;
}
__device__ __forceinline__ f32x2 unpk_sw(unsigned u) {
  f32x2 r;
  r.x = __uint_as_float(u & 0xffff0000u);
  r.y = __uint_as_float(u << 16);
  return r;
}
__device__ __forceinline__ f32x2 rn2(f32x2 v) { return unpk(cvtpk(v)); }
__device__ __forceinline__ unsigned dw(const u32x4& v, int i) {
  return (i == 0) ? v.x : (i == 1) ? v.y : (i == 2) ? v.z : v.w;
}

#define WAIT_V(n) asm volatile("s_waitcnt vmcnt(" #n ") lgkmcnt(0)" ::: "memory")
#define WAIT_L() asm volatile("s_waitcnt lgkmcnt(0)" ::: "memory")
#define BAR() __builtin_amdgcn_s_barrier()
#define SCHED() __builtin_amdgcn_sched_barrier(0)
#define SGB(mask, n) __builtin_amdgcn_sched_group_barrier(mask, n, 0)

__device__ __forceinline__ void glds16(const void* sbase, unsigned voff, unsigned lds_dst) {
  unsigned keep;
  asm volatile(
      "s_mov_b32 %0, m0\n\ts_mov_b32 m0, %3\n\ts_nop 0\n\tglobal_load_lds_dwordx4 %1, %2\n\ts_mov_b32 m0, %0"
      : "=&s"(keep)
      : "v"(voff), "s"(sbase), "s"(lds_dst)
      : "memory");
}
__device__ __forceinline__ void touch(const void* p) {
  unsigned t;
  asm volatile("global_load_dword %0, %1, off\n\ts_waitcnt vmcnt(0)" : "=v"(t) : "v"(p) : "memory");
}

static constexpr int G1_NS = 5;
static constexpr int G1_CT = 128 * 128;
static constexpr int G1_XT = 64 * 256;
static constexpr int G1_STAGE = G1_CT + G1_XT;
static constexpr int NGEMM1 = 160;
static constexpr int NHELP = 96;

struct G1Slice {
  bf16x8 cf[2][2];
  bf16x8 xf[2][2];
};

__device__ __forceinline__ void big_gemm(char* smem, int b, const u16* __restrict__ Cre,
                                         const u16* __restrict__ Cim, const u16* __restrict__ x,
                                         u16* __restrict__ PQ) {
  const int tid = threadIdx.x;
  const int w = __builtin_amdgcn_readfirstlane(tid >> 6);
  const int lane = tid & 63;
  const int xcd = b & 7, idx = b >> 3;
  const int ttile = xcd * 4 + idx / 5;
  const int dtile = idx % 5;
  const int t0 = ttile * 128;
  const int is_im = t0 >= S_LEN;
  const int trow0 = t0 & (S_LEN - 1);
  const int d0 = dtile * 128;
  auto nxt = [](int bb) { return (bb + 1 == G1_NS) ? 0 : bb + 1; };
  const int g = lane >> 4, r16 = lane & 15;

  if (w >= 4) {
    const int lw = w - 4;
    const u16* Cp = (is_im ? Cim : Cre) + (size_t)trow0 * S_LEN;
    const u16* xp = x + d0;
    const unsigned lds0 = __builtin_amdgcn_readfirstlane((unsigned)(size_t)smem);
    unsigned coff[2], xoff[2];
#pragma unroll
    for (int i = 0; i < 2; ++i) {
      int row = 16 * lw + 8 * i + (lane >> 3);
      int sc = (lane & 7) ^ ((row >> 1) & 7);
      coff[i] = (row * S_LEN + 8 * sc) * 2;
      int xr = 8 * lw + 4 * i + (lane >> 4);
      int xc = (lane & 15) ^ ((xr & 3) << 2);
      xoff[i] = (xr * D_DIM + 8 * xc) * 2;
    }
    auto stage_issue = [&](int step, int buf) {
      unsigned sb = lds0 + buf * G1_STAGE;
      const u16* cg = Cp + step * 64;
      const u16* xg = xp + (size_t)step * 64 * D_DIM;
#pragma unroll
      for (int i = 0; i < 2; ++i) {
        glds16(cg, coff[i], sb + (16 * lw + 8 * i) * 128);
        glds16(xg, xoff[i], sb + G1_CT + (8 * lw + 4 * i) * 256);
      }
    };
    stage_issue(0, 0);
    stage_issue(1, 1);
    WAIT_V(4);
    BAR();
#pragma unroll
    for (int s = 2; s < G1_NS; ++s) stage_issue(s, s);
    int bk = 0;
    for (int k = 0; k < 27; ++k) {
      WAIT_V(12);
      BAR();
      stage_issue(k + G1_NS, bk);
      bk = nxt(bk);
    }
    WAIT_V(12); BAR();
    WAIT_V(8);  BAR();
    WAIT_V(4);  BAR();
    WAIT_V(0);  BAR();
  } else {
    const int wt = w >> 1, wd = w & 1;
    const int r32 = lane & 31, h = lane >> 5;
    int crd[4];
#pragma unroll
    for (int kq = 0; kq < 4; ++kq) crd[kq] = (64 * wt + r32) * 128 + 16 * ((2 * kq + h) ^ ((r32 >> 1) & 7));
    const int q4 = (lane & 15) >> 2, p4 = lane & 3;
    int xrd[2];
#pragma unroll
    for (int dt = 0; dt < 2; ++dt)
      xrd[dt] = G1_CT + (8 * h + q4) * 256 + 64 * ((2 * wd + dt) ^ q4) + 32 * ((lane >> 4) & 1) + 8 * p4;

    f32x16 acc[2][2];
#pragma unroll
    for (int i = 0; i < 2; ++i)
#pragma unroll
      for (int j = 0; j < 2; ++j)
#pragma unroll
        for (int e = 0; e < 16; ++e) acc[i][j][e] = 0.f;

    auto load_slice = [&](G1Slice& f, int buf, int ks) {
      const char* sb = smem + buf * G1_STAGE;
#pragma unroll
      for (int kk = 0; kk < 2; ++kk) {
        const int kq = 2 * ks + kk;
#pragma unroll
        for (int dt = 0; dt < 2; ++dt) {
          s16x4 lo = __builtin_amdgcn_ds_read_tr16_b64_v4i16((LDS_AS s16x4*)(sb + xrd[dt] + (16 * kq) * 256));
          s16x4 hi = __builtin_amdgcn_ds_read_tr16_b64_v4i16((LDS_AS s16x4*)(sb + xrd[dt] + (16 * kq + 4) * 256));
          f.xf[kk][dt] = __builtin_shufflevector(lo, hi, 0, 1, 2, 3, 4, 5, 6, 7);
        }
#pragma unroll
        for (int tt = 0; tt < 2; ++tt) f.cf[kk][tt] = *(const bf16x8*)(sb + crd[kq] + tt * 4096);
      }
    };
    auto mma = [&](const G1Slice& f) {
#pragma unroll
      for (int kk = 0; kk < 2; ++kk)
#pragma unroll
        for (int dt = 0; dt < 2; ++dt)
#pragma unroll
          for (int tt = 0; tt < 2; ++tt)
            acc[dt][tt] = __builtin_amdgcn_mfma_f32_32x32x16_bf16(f.xf[kk][dt], f.cf[kk][tt], acc[dt][tt], 0, 0, 0);
    };
#define ILV12_8() do { _Pragma("unroll") for (int _i = 0; _i < 4; ++_i) { SGB(0x8, 1); SGB(0x100, 3); } SGB(0x8, 4); } while (0)
    G1Slice f0, f1;
    BAR();
    load_slice(f0, 0, 0);
    int bk = 0;
    for (int k = 0; k < 31; ++k) {
      SCHED(); load_slice(f1, bk, 1); mma(f0); ILV12_8(); SCHED();
      WAIT_L();
      BAR();
      bk = nxt(bk);
      SCHED(); load_slice(f0, bk, 0); mma(f1); ILV12_8(); SCHED();
    }
    SCHED(); load_slice(f1, bk, 1); mma(f0); ILV12_8(); SCHED();
    mma(f1);
    WAIT_L();
    const float sgn = is_im ? -1.f : 1.f;
    char* ob = smem + 3 * G1_STAGE;
#pragma unroll
    for (int tt = 0; tt < 2; ++tt)
#pragma unroll
      for (int dt = 0; dt < 2; ++dt)
#pragma unroll
        for (int rq = 0; rq < 4; ++rq) {
          u32x2 v;
          v.x = pack2(sgn * acc[dt][tt][4 * rq + 0], sgn * acc[dt][tt][4 * rq + 1]);
          v.y = pack2(sgn * acc[dt][tt][4 * rq + 2], sgn * acc[dt][tt][4 * rq + 3]);
          *(u32x2*)(ob + (64 * wt + 32 * tt + r32) * 272 + (64 * wd + 32 * dt + 8 * rq + 4 * h) * 2) = v;
        }
  }
  __syncthreads();
  {
    const char* ob = smem + 3 * G1_STAGE;
    u16* outp = PQ + (size_t)trow0 * (2 * D_DIM) + (is_im ? D_DIM : 0) + d0;
#pragma unroll
    for (int i = 0; i < 3; ++i) {
      int row = 4 * (w + 12 * i) + g;
      if (row < 128) {
        u32x4 v = *(const u32x4*)(ob + row * 272 + r16 * 16);
        *(u32x4*)(outp + (size_t)row * (2 * D_DIM) + r16 * 8) = v;
      }
    }
  }
}

__device__ __forceinline__ void helper_block(int hb, const u16* __restrict__ Cre, const u16* __restrict__ Cim,
                                             const u16* __restrict__ x, const u16* __restrict__ q,
                                             const u16* __restrict__ k, const u16* __restrict__ v,
                                             const u16* __restrict__ shk, const u16* __restrict__ ssmk,
                                             const u16* __restrict__ Wre, const u16* __restrict__ Wim,
                                             u16* __restrict__ kvT) {
  const int w = __builtin_amdgcn_readfirstlane(threadIdx.x >> 6);
  const int lane = threadIdx.x & 63;
  const int xcd = hb & 7;
  const int gw = hb * 4 + w;
  if (w >= 4) {
    const int wi = (hb >> 3) * 8 + (w - 4);
    constexpr int NW = NHELP / 8 * 8;
#pragma unroll 1
    for (int u = wi; u < 32 * 18; u += NW) {
      const int step = u / 18, n = u - step * 18;
      const char* p;
      if (n < 8) {
        int row = 512 * xcd + 64 * n + lane;
        const u16* cb = (row >= S_LEN) ? Cim : Cre;
        p = (const char*)(cb + (size_t)(row & (S_LEN - 1)) * S_LEN) + step * 128;
      } else {
        p = (const char*)x + (size_t)step * (64 * D_DIM * 2) + (n - 8) * 8192 + lane * 128;
      }
      touch(p);
    }
#pragma unroll 1
    for (int u = hb * 8 + (w - 4); u < 840; u += NHELP * 8) {
      const char* p;
      if (u < 320) p = (const char*)q + (size_t)u * 8192;
      else if (u < 640) p = (const char*)ssmk + (size_t)(u - 320) * 8192;
      else if (u < 740) p = (const char*)Wre + (size_t)(u - 640) * 8192;
      else p = (const char*)Wim + (size_t)(u - 740) * 8192;
      touch(p + lane * 128);
    }
  } else if (gw < 320) {
    const int s0 = (gw / 10) * 64, c0 = (gw % 10) * 64;
    const int rg = lane >> 3, cc = lane & 7;
    const int d = c0 + 8 * cc;
    const int r0 = s0 + 8 * rg;
    u32x4 kr[8], vr[8], kh[3];
#pragma unroll
    for (int r = 0; r < 8; ++r) kr[r] = *(const u32x4*)(k + (size_t)(r0 + r) * D_DIM + d);
#pragma unroll
    for (int j = 0; j < 3; ++j) {
      if (s0 == 0) kh[j] = (u32x4){0u, 0u, 0u, 0u};
      else kh[j] = *(const u32x4*)(k + (size_t)(s0 - 3 + j) * D_DIM + d);
    }
#pragma unroll
    for (int r = 0; r < 8; ++r) vr[r] = *(const u32x4*)(v + (size_t)(r0 + r) * D_DIM + d);
    u32x4 sk[4];
#pragma unroll
    for (int cp = 0; cp < 4; ++cp) sk[cp] = *(const u32x4*)(shk + (size_t)(d + 2 * cp) * 4);
#pragma unroll
    for (int j = 0; j < 3; ++j) {
      u32x4 t;
      t.x = __shfl_up(kr[5 + j].x, 8, 64);
      t.y = __shfl_up(kr[5 + j].y, 8, 64);
      t.z = __shfl_up(kr[5 + j].z, 8, 64);
      t.w = __shfl_up(kr[5 + j].w, 8, 64);
      if (rg != 0) kh[j] = t;
    }
#pragma unroll
    for (int cp = 0; cp < 4; ++cp) {
      f32x2 s0v, s1v, s2v, s3v;
      s0v.x = bf2f(sk[cp].x & 0xffffu); s1v.x = bf2f(sk[cp].x >> 16); s2v.x = bf2f(sk[cp].y & 0xffffu); s3v.x = bf2f(sk[cp].y >> 16);
      s0v.y = bf2f(sk[cp].z & 0xffffu); s1v.y = bf2f(sk[cp].z >> 16); s2v.y = bf2f(sk[cp].w & 0xffffu); s3v.y = bf2f(sk[cp].w >> 16);
      f32x2 K[11];
#pragma unroll
      for (int j = 0; j < 3; ++j) K[j] = unpk(dw(kh[j], cp));
#pragma unroll
      for (int r = 0; r < 8; ++r) K[3 + r] = unpk(dw(kr[r], cp));
      unsigned o[8];
#pragma unroll
      for (int r = 0; r < 8; ++r) {
        f32x2 a = rn2(K[r] * s3v);
        a = rn2(a + rn2(K[r + 1] * s2v));
        a = rn2(a + rn2(K[r + 2] * s1v));
        a = rn2(a + rn2(K[r + 3] * s0v));
        o[r] = cvtpk(a * unpk(dw(vr[r], cp)));
      }
      u32x4 lo, hi;
      lo.x = __builtin_amdgcn_perm(o[6], o[7], 0x05040100u); hi.x = __builtin_amdgcn_perm(o[6], o[7], 0x07060302u);
      lo.y = __builtin_amdgcn_perm(o[4], o[5], 0x05040100u); hi.y = __builtin_amdgcn_perm(o[4], o[5], 0x07060302u);
      lo.z = __builtin_amdgcn_perm(o[2], o[3], 0x05040100u); hi.z = __builtin_amdgcn_perm(o[2], o[3], 0x07060302u);
      lo.w = __builtin_amdgcn_perm(o[0], o[1], 0x05040100u); hi.w = __builtin_amdgcn_perm(o[0], o[1], 0x07060302u);
      *(u32x4*)(kvT + (size_t)(d + 2 * cp) * S_LEN + r0) = lo;
      *(u32x4*)(kvT + (size_t)(d + 2 * cp + 1) * S_LEN + r0) = hi;
    }
  }
}

static constexpr int K1_SMEM = G1_NS * G1_STAGE;

__global__ void __launch_bounds__(768, 3)
k1_kernel(const u16* __restrict__ Cre, const u16* __restrict__ Cim, const u16* __restrict__ x,
          u16* __restrict__ PQ, const u16* __restrict__ q, const u16* __restrict__ k,
          const u16* __restrict__ v, const u16* __restrict__ shk, const u16* __restrict__ ssmk,
          const u16* __restrict__ Wre, const u16* __restrict__ Wim, u16* __restrict__ kvT) {
  __shared__ __attribute__((aligned(16))) char smem[K1_SMEM];
  const int b = blockIdx.x;
  if (b < NGEMM1) big_gemm(smem, b, Cre, Cim, x, PQ);
  else helper_block(b - NGEMM1, Cre, Cim, x, q, k, v, shk, ssmk, Wre, Wim, kvT);
}

static constexpr int NCONV = 160;
static constexpr int CV_XS = (256 + 2048) * 2;
static constexpr int CV_HC = 2080 * 2;
static constexpr int CV_WAVE = CV_XS + 2 * CV_HC;

__device__ __forceinline__ void conv_block(char* smem, int cb, const u16* __restrict__ q,
                                           const u16* __restrict__ kvT, const u16* __restrict__ ssmk,
                                           const u16* __restrict__ ssmD, u16* __restrict__ out,
                                           const u16* __restrict__ PQ, const u16* __restrict__ Wre,
                                           const u16* __restrict__ Wim) {
  const int tid = threadIdx.x;
  const int w = __builtin_amdgcn_readfirstlane(tid >> 6);
  const int lane = tid & 63;
  const int grp = (cb & 7) * 20 + (cb >> 3);
  const int d0 = grp * 4;
  const int r0 = 4 * tid;
  u32x2 qv[4];
  auto qload = [&](int i) { qv[i] = *(const u32x2*)(q + (size_t)(r0 + i) * D_DIM + d0); };

  if (w >= 4) {
    const int xcd = cb & 7;
    const int wi = (cb >> 3) * 8 + (w - 4);
#pragma unroll 1
    for (int u = wi; u < 20 * 14; u += 160) {
      const int step = u / 14, n = u - step * 14;
      const char* p;
      if (n < 4) p = (const char*)(PQ + (size_t)(256 * xcd + 64 * n + lane) * (2 * D_DIM)) + step * 128;
      else {
        const u16* wb = (step < 10) ? Wre : Wim;
        p = (const char*)(wb + (size_t)(64 * (n - 4) + lane) * D_DIM) + (step < 10 ? step : step - 10) * 128;
      }
      touch(p);
    }
    if (w < 8) {
#pragma unroll
      for (int i = 0; i < 4; ++i) qload(i);
    }
  } else {
    const float Dv = bf2f(ssmD[d0 + w]);
    char* xs = smem + w * CV_WAVE;
    char* hc0 = xs + CV_XS;
    {
      u32x4 xv[4], hv[4];
      const u16* xg = kvT + (size_t)(d0 + w) * S_LEN;
      const u16* hg = ssmk + (size_t)(d0 + w) * S_LEN;
#pragma unroll
      for (int i = 0; i < 4; ++i) {
        xv[i] = *(const u32x4*)(xg + 512 * i + 8 * lane);
        hv[i] = *(const u32x4*)(hg + 512 * i + 8 * lane);
      }
      char* hc1 = hc0 + CV_HC;
#pragma unroll
      for (int i = 0; i < 4; ++i) {
        *(u32x4*)(xs + (256 + 512 * i + 8 * lane) * 2) = xv[i];
        unsigned nx = __shfl_down(hv[i].x, 1, 64);
        unsigned nxt_first = (i < 3) ? __shfl(hv[(i < 3) ? i + 1 : 3].x, 0, 64) : 0u;
        if (lane == 63) nx = nxt_first;
        u32x4 sh;
        sh.x = __builtin_amdgcn_alignbyte(hv[i].y, hv[i].x, 2);
        sh.y = __builtin_amdgcn_alignbyte(hv[i].z, hv[i].y, 2);
        sh.z = __builtin_amdgcn_alignbyte(hv[i].w, hv[i].z, 2);
        sh.w = __builtin_amdgcn_alignbyte(nx, hv[i].w, 2);
        *(u32x4*)(hc0 + 32 + (512 * i + 8 * lane) * 2) = hv[i];
        *(u32x4*)(hc1 + 32 + (512 * i + 8 * lane) * 2) = sh;
      }
      unsigned h0 = __shfl(hv[0].x, 0, 64);
      if (lane < 2) *(u32x4*)(hc0 + 16 * lane) = (u32x4){0u, 0u, 0u, 0u};
      if (lane == 2) *(u32x4*)(hc1) = (u32x4){0u, 0u, 0u, 0u};
      if (lane == 3) *(u32x4*)(hc1 + 16) = (u32x4){0u, 0u, 0u, (h0 & 0xffffu) << 16};
      if (lane < 32) *(u32x4*)(xs + 16 * lane) = (u32x4){0u, 0u, 0u, 0u};
    }
    WAIT_L();

    const int g = lane >> 4, r16 = lane & 15;
    const char* abase = (r16 & 1) ? (hc0 + 2 * (25 + r16 - 8 * g)) : (hc0 + CV_HC + 2 * (24 + r16 - 8 * g));
    const char* bbase = xs + 2 * (240 + 16 * r16 + 8 * g);
    f32x4 acc[8];
#pragma unroll
    for (int i = 0; i < 8; ++i) acc[i] = (f32x4){0.f, 0.f, 0.f, 0.f};
    struct CvFrags { bf16x8 af[8], bfm[8]; };
    auto cv_load = [&](CvFrags& f, int rho) {
#pragma unroll
      for (int u = 0; u < 8; ++u) {
        const unsigned* ap = (const unsigned*)(abase + 64 * (8 * u) + 64 * rho);
        u32x4 t;
        t.x = ap[0]; t.y = ap[1]; t.z = ap[2]; t.w = ap[3];
        f.af[u] = __builtin_bit_cast(bf16x8, t);
      }
#pragma unroll
      for (int J = 0; J < 8; ++J) f.bfm[J] = *(const bf16x8*)(bbase + 512 * J - 64 * rho);
    };
    auto cv_mma = [&](const CvFrags& f) {
#pragma unroll
      for (int J = 0; J < 8; ++J)
#pragma unroll
        for (int I = J; I < 8; ++I)
          acc[I] = __builtin_amdgcn_mfma_f32_16x16x32_bf16(f.af[I - J], f.bfm[J], acc[I], 0, 0, 0);
    };
#define ILV24_36() do { _Pragma("unroll") for (int _i = 0; _i < 24; ++_i) { SGB(0x100, 1); SGB(0x8, 1); } SGB(0x8, 12); } while (0)
    {
      CvFrags ca, cb2;
      cv_load(ca, 0);
#pragma unroll
      for (int rho = 0; rho < 6; rho += 2) {
        qload(rho >> 1);
        SCHED(); cv_load(cb2, rho + 1); cv_mma(ca); ILV24_36(); SCHED();
        SCHED(); cv_load(ca, rho + 2); cv_mma(cb2); ILV24_36(); SCHED();
      }
      qload(3);
      SCHED(); cv_load(cb2, 7); cv_mma(ca); ILV24_36(); SCHED();
      cv_mma(cb2);
    }

    {
      const f32x2 D2 = {Dv, Dv};
#pragma unroll
      for (int I = 0; I < 8; ++I) {
        int n = 256 * I + 16 * r16 + 4 * g;
        u32x2 kvp = *(const u32x2*)(xs + 2 * (256 + (n & ~7) + 4 - (n & 7)));
        f32x2 a01 = {acc[I][0], acc[I][1]}, a23 = {acc[I][2], acc[I][3]};
        u32x2 o;
        o.x = cvtpk(rn2(a01) + rn2(unpk_sw(kvp.y) * D2));
        o.y = cvtpk(rn2(a23) + rn2(unpk_sw(kvp.x) * D2));
        *(u32x2*)(hc0 + 2 * n) = o;
      }
    }
  }
  __syncthreads();

  if (w < 8) {
    u32x2 zc[4];
#pragma unroll
    for (int c = 0; c < 4; ++c) zc[c] = *(const u32x2*)(smem + c * CV_WAVE + CV_XS + 2 * r0);
#pragma unroll
    for (int r = 0; r < 4; ++r) {
      unsigned o[2];
#pragma unroll
      for (int cp = 0; cp < 2; ++cp) {
        unsigned za = (r < 2) ? zc[2 * cp].x : zc[2 * cp].y;
        unsigned zb = (r < 2) ? zc[2 * cp + 1].x : zc[2 * cp + 1].y;
        f32x2 z;
        z.x = __uint_as_float((r & 1) ? (za & 0xffff0000u) : (za << 16));
        z.y = __uint_as_float((r & 1) ? (zb & 0xffff0000u) : (zb << 16));
        o[cp] = cvtpk(unpk(cp ? qv[r].y : qv[r].x) * z);
      }
      u32x2 ov = {o[0], o[1]};
      *(u32x2*)(out + (size_t)(r0 + r) * (2 * D_DIM) + d0) = ov;
    }
  }
}

static constexpr int G2_NS = 5;
static constexpr int G2_PT = 128 * 128;
static constexpr int G2_WT = 128 * 128;
static constexpr int G2_STAGE = G2_PT + G2_WT;
static constexpr int NGEMM2 = 80;

struct G2Slice {
  bf16x8 pf[2][2];
  bf16x8 wf[2][2];
};

__device__ __forceinline__ void small_gemm(char* smem, int b, const u16* __restrict__ PQ,
                                           const u16* __restrict__ Wre, const u16* __restrict__ Wim,
                                           u16* __restrict__ out) {
  const int tid = threadIdx.x;
  const int w = __builtin_amdgcn_readfirstlane(tid >> 6);
  const int lane = tid & 63;
  const int xcd = b & 7, idx = b >> 3;
  const int ttile = xcd * 2 + idx / 5;
  const int etile = idx % 5;
  const int t0 = ttile * 128, e0 = etile * 128;
  auto nxt = [](int bb) { return (bb + 1 == G2_NS) ? 0 : bb + 1; };
  const int g = lane >> 4, r16 = lane & 15;

  if (w >= 4) {
    const int lw = w - 4;
    const unsigned lds0 = __builtin_amdgcn_readfirstlane((unsigned)(size_t)smem);
    unsigned poff[2], woff[2];
#pragma unroll
    for (int i = 0; i < 2; ++i) {
      int row = 16 * lw + 8 * i + (lane >> 3);
      int sc = (lane & 7) ^ ((row >> 1) & 7);
      poff[i] = ((t0 + row) * (2 * D_DIM) + 8 * sc) * 2;
      woff[i] = ((e0 + row) * D_DIM + 8 * sc) * 2;
    }
    auto stage_issue = [&](int step, int buf) {
      unsigned sb = lds0 + buf * G2_STAGE;
      const u16* pg = PQ + step * 64;
      const u16* wg = (step < 10) ? (Wre + step * 64) : (Wim + (step - 10) * 64);
#pragma unroll
      for (int i = 0; i < 2; ++i) {
        glds16(pg, poff[i], sb + (16 * lw + 8 * i) * 128);
        glds16(wg, woff[i], sb + G2_PT + (16 * lw + 8 * i) * 128);
      }
    };
    stage_issue(0, 0);
    stage_issue(1, 1);
    WAIT_V(4);
    BAR();
#pragma unroll
    for (int s = 2; s < G2_NS; ++s) stage_issue(s, s);
    int bk = 0;
    for (int k = 0; k < 15; ++k) {
      WAIT_V(12);
      BAR();
      stage_issue(k + G2_NS, bk);
      bk = nxt(bk);
    }
    WAIT_V(12); BAR();
    WAIT_V(8);  BAR();
    WAIT_V(4);  BAR();
    WAIT_V(0);  BAR();
  } else {
    const int wt = w >> 1, we = w & 1;
    const int r32 = lane & 31, h = lane >> 5;
    int prd[4], wrd[4];
#pragma unroll
    for (int kq = 0; kq < 4; ++kq) {
      int sw = 16 * ((2 * kq + h) ^ ((r32 >> 1) & 7));
      prd[kq] = (64 * wt + r32) * 128 + sw;
      wrd[kq] = G2_PT + (64 * we + r32) * 128 + sw;
    }
    f32x16 acc[2][2];
#pragma unroll
    for (int i = 0; i < 2; ++i)
#pragma unroll
      for (int j = 0; j < 2; ++j)
#pragma unroll
        for (int e = 0; e < 16; ++e) acc[i][j][e] = 0.f;
    auto load_slice = [&](G2Slice& f, int buf, int ks) {
      const char* sb = smem + buf * G2_STAGE;
#pragma unroll
      for (int kk = 0; kk < 2; ++kk)
#pragma unroll
        for (int i = 0; i < 2; ++i) {
          f.wf[kk][i] = *(const bf16x8*)(sb + wrd[2 * ks + kk] + i * 4096);
          f.pf[kk][i] = *(const bf16x8*)(sb + prd[2 * ks + kk] + i * 4096);
        }
    };
    auto mma = [&](const G2Slice& f) {
#pragma unroll
      for (int kk = 0; kk < 2; ++kk)
#pragma unroll
        for (int et = 0; et < 2; ++et)
#pragma unroll
          for (int tt = 0; tt < 2; ++tt)
            acc[et][tt] = __builtin_amdgcn_mfma_f32_32x32x16_bf16(f.wf[kk][et], f.pf[kk][tt], acc[et][tt], 0, 0, 0);
    };
#define ILV8_8() do { _Pragma("unroll") for (int _i = 0; _i < 4; ++_i) { SGB(0x8, 1); SGB(0x100, 2); } SGB(0x8, 4); } while (0)
    G2Slice f0, f1;
    BAR();
    load_slice(f0, 0, 0);
    int bk = 0;
    for (int k = 0; k < 19; ++k) {
      SCHED(); load_slice(f1, bk, 1); mma(f0); ILV8_8(); SCHED();
      WAIT_L();
      BAR();
      bk = nxt(bk);
      SCHED(); load_slice(f0, bk, 0); mma(f1); ILV8_8(); SCHED();
    }
    SCHED(); load_slice(f1, bk, 1); mma(f0); ILV8_8(); SCHED();
    mma(f1);
    WAIT_L();
    char* ob = smem;
#pragma unroll
    for (int et = 0; et < 2; ++et)
#pragma unroll
      for (int tt = 0; tt < 2; ++tt)
#pragma unroll
        for (int rq = 0; rq < 4; ++rq) {
          u32x2 v;
          v.x = pack2(acc[et][tt][4 * rq + 0], acc[et][tt][4 * rq + 1]);
          v.y = pack2(acc[et][tt][4 * rq + 2], acc[et][tt][4 * rq + 3]);
          *(u32x2*)(ob + (64 * wt + 32 * tt + r32) * 272 + (64 * we + 32 * et + 8 * rq + 4 * h) * 2) = v;
        }
  }
  __syncthreads();
  {
    const char* ob = smem;
    u16* op = out + (size_t)t0 * (2 * D_DIM) + D_DIM + e0;
#pragma unroll
    for (int i = 0; i < 3; ++i) {
      int row = 4 * (w + 12 * i) + g;
      if (row < 128) {
        u32x4 v = *(const u32x4*)(ob + row * 272 + r16 * 16);
        *(u32x4*)(op + (size_t)row * (2 * D_DIM) + r16 * 8) = v;
      }
    }
  }
}

static constexpr int K2_SMEM = G2_NS * G2_STAGE;
static_assert(K2_SMEM >= 4 * CV_WAVE, "lds");

__global__ void __launch_bounds__(768, 3)
k2_kernel(const u16* __restrict__ PQ, const u16* __restrict__ Wre, const u16* __restrict__ Wim,
          const u16* __restrict__ q, const u16* __restrict__ kvT, const u16* __restrict__ ssmk,
          const u16* __restrict__ ssmD, u16* __restrict__ out) {
  __shared__ __attribute__((aligned(16))) char smem[K2_SMEM];
  const int b = blockIdx.x;
  if (b < NGEMM2) small_gemm(smem, b, PQ, Wre, Wim, out);
  else conv_block(smem, b - NGEMM2, q, kvT, ssmk, ssmD, out, PQ, Wre, Wim);
}

extern "C" void kernel_launch(void* const* d_in, const int* in_sizes, int n_in, void* d_out,
                              int out_size, void* d_ws, size_t ws_size, hipStream_t stream) {
  const u16* q = (const u16*)d_in[0];
  const u16* k = (const u16*)d_in[1];
  const u16* v = (const u16*)d_in[2];
  const u16* shk = (const u16*)d_in[3];
  const u16* ssmk = (const u16*)d_in[4];
  const u16* ssmD = (const u16*)d_in[5];
  const u16* x = (const u16*)d_in[6];
  const u16* Cre = (const u16*)d_in[7];
  const u16* Cim = (const u16*)d_in[8];
  const u16* Wre = (const u16*)d_in[9];
  const u16* Wim = (const u16*)d_in[10];
  u16* out = (u16*)d_out;
  u16* PQ = (u16*)d_ws;
  u16* kvT = PQ + (size_t)S_LEN * 2 * D_DIM;

  k1_kernel<<<NGEMM1 + NHELP, 768, 0, stream>>>(Cre, Cim, x, PQ, q, k, v, shk, ssmk, Wre, Wim, kvT);
  k2_kernel<<<NCONV + NGEMM2, 768, 0, stream>>>(PQ, Wre, Wim, q, kvT, ssmk, ssmD, out);
}
```

```cpp
#include <hip/hip_runtime.h>
#include <hip/hip_bf16.h>
#include <stdint.h>

typedef __attribute__((ext_vector_type(8))) short bf16x8;
typedef __attribute__((ext_vector_type(4))) short s16x4;
typedef __attribute__((ext_vector_type(4))) float f32x4;
typedef __attribute__((ext_vector_type(16))) float f32x16;
typedef __attribute__((ext_vector_type(2))) float f32x2;
typedef __attribute__((ext_vector_type(2))) __bf16 bf16x2v;
typedef __attribute__((ext_vector_type(4))) unsigned u32x4;
typedef __attribute__((ext_vector_type(2))) unsigned u32x2;
typedef unsigned short u16;

#define LDS_AS __attribute__((address_space(3)))

static constexpr int S_LEN = 2048;
static constexpr int D_DIM = 640;

__device__ __forceinline__ float bf2f(unsigned h) { return __uint_as_float(h << 16); }
__device__ __forceinline__ unsigned cvtpk(f32x2 v) {
  bf16x2v b = __builtin_convertvector(v, bf16x2v);
  return __builtin_bit_cast(unsigned, b);
}
__device__ __forceinline__ unsigned pack2(float lo, float hi) { return cvtpk((f32x2){lo, hi}); }
__device__ __forceinline__ f32x2 unpk(unsigned u) {
  f32x2 r;
  r.x = __uint_as_float(u << 16);
  r.y = __uint_as_float(u & 0xffff0000u);
  return r;
}
__device__ __forceinline__ f32x2 unpk_sw(unsigned u) {
  f32x2 r;
  r.x = __uint_as_float(u & 0xffff0000u);
  r.y = __uint_as_float(u << 16);
  return r;
}
__device__ __forceinline__ f32x2 rn2(f32x2 v) { return unpk(cvtpk(v)); }
__device__ __forceinline__ unsigned dw(const u32x4& v, int i) {
  return (i == 0) ? v.x : (i == 1) ? v.y : (i == 2) ? v.z : v.w;
}

#define WAIT_V(n) asm volatile("s_waitcnt vmcnt(" #n ") lgkmcnt(0)" ::: "memory")
#define WAIT_L() asm volatile("s_waitcnt lgkmcnt(0)" ::: "memory")
#define BAR() __builtin_amdgcn_s_barrier()
#define SCHED() __builtin_amdgcn_sched_barrier(0)
#define SGB(mask, n) __builtin_amdgcn_sched_group_barrier(mask, n, 0)

__device__ __forceinline__ void glds16(const void* sbase, unsigned voff, unsigned lds_dst) {
  unsigned keep;
  asm volatile(
      "s_mov_b32 %0, m0\n\ts_mov_b32 m0, %3\n\ts_nop 0\n\tglobal_load_lds_dwordx4 %1, %2\n\ts_mov_b32 m0, %0"
      : "=&s"(keep)
      : "v"(voff), "s"(sbase), "s"(lds_dst)
      : "memory");
}
__device__ __forceinline__ void touch(const void* p) {
  unsigned t;
  asm volatile("global_load_dword %0, %1, off\n\ts_waitcnt vmcnt(0)" : "=v"(t) : "v"(p) : "memory");
}

static constexpr int G1_NS = 5;
static constexpr int G1_CT = 128 * 128;
static constexpr int G1_XT = 64 * 256;
static constexpr int G1_STAGE = G1_CT + G1_XT;
static constexpr int NGEMM1 = 160;
static constexpr int NHELP = 96;

struct G1Slice {
  bf16x8 cf[2][2];
  bf16x8 xf[2][2];
};

__device__ __forceinline__ void big_gemm(char* smem, int b, const u16* __restrict__ Cre,
                                         const u16* __restrict__ Cim, const u16* __restrict__ x,
                                         u16* __restrict__ PQ) {
  const int tid = threadIdx.x;
  const int w = __builtin_amdgcn_readfirstlane(tid >> 6);
  const int lane = tid & 63;
  const int xcd = b & 7, idx = b >> 3;
  const int tq = idx / 5;
  const int ttile = (tq >> 1) * 16 + 2 * xcd + (tq & 1);
  const int dtile = idx % 5;
  const int t0 = ttile * 128;
  const int is_im = t0 >= S_LEN;
  const int trow0 = t0 & (S_LEN - 1);
  const int d0 = dtile * 128;
  auto nxt = [](int bb) { return (bb + 1 == G1_NS) ? 0 : bb + 1; };
  const int g = lane >> 4, r16 = lane & 15;

  if (w >= 4) {
    const int lw = w - 4;
    const u16* Cp = (is_im ? Cim : Cre) + (size_t)trow0 * S_LEN;
    const u16* xp = x + d0;
    const unsigned lds0 = __builtin_amdgcn_readfirstlane((unsigned)(size_t)smem);
    unsigned coff[2], xoff[2];
#pragma unroll
    for (int i = 0; i < 2; ++i) {
      int row = 16 * lw + 8 * i + (lane >> 3);
      int sc = (lane & 7) ^ ((row >> 1) & 7);
      coff[i] = (row * S_LEN + 8 * sc) * 2;
      int xr = 8 * lw + 4 * i + (lane >> 4);
      int xc = (lane & 15) ^ ((xr & 3) << 2);
      xoff[i] = (xr * D_DIM + 8 * xc) * 2;
    }
    auto stage_issue = [&](int step, int buf) {
      unsigned sb = lds0 + buf * G1_STAGE;
      const u16* cg = Cp + step * 64;
      const u16* xg = xp + (size_t)step * 64 * D_DIM;
#pragma unroll
      for (int i = 0; i < 2; ++i) {
        glds16(cg, coff[i], sb + (16 * lw + 8 * i) * 128);
        glds16(xg, xoff[i], sb + G1_CT + (8 * lw + 4 * i) * 256);
      }
    };
    stage_issue(0, 0);
    stage_issue(1, 1);
    WAIT_V(4);
    BAR();
#pragma unroll
    for (int s = 2; s < G1_NS; ++s) stage_issue(s, s);
    int bk = 0;
    for (int k = 0; k < 27; ++k) {
      WAIT_V(12);
      BAR();
      stage_issue(k + G1_NS, bk);
      bk = nxt(bk);
    }
    WAIT_V(12); BAR();
    WAIT_V(8);  BAR();
    WAIT_V(4);  BAR();
    WAIT_V(0);  BAR();
  } else {
    const int wt = w >> 1, wd = w & 1;
    const int r32 = lane & 31, h = lane >> 5;
    int crd[4];
#pragma unroll
    for (int kq = 0; kq < 4; ++kq) crd[kq] = (64 * wt + r32) * 128 + 16 * ((2 * kq + h) ^ ((r32 >> 1) & 7));
    const int q4 = (lane & 15) >> 2, p4 = lane & 3;
    int xrd[2];
#pragma unroll
    for (int dt = 0; dt < 2; ++dt)
      xrd[dt] = G1_CT + (8 * h + q4) * 256 + 64 * ((2 * wd + dt) ^ q4) + 32 * ((lane >> 4) & 1) + 8 * p4;

    f32x16 acc[2][2];
#pragma unroll
    for (int i = 0; i < 2; ++i)
#pragma unroll
      for (int j = 0; j < 2; ++j)
#pragma unroll
        for (int e = 0; e < 16; ++e) acc[i][j][e] = 0.f;

    auto load_slice = [&](G1Slice& f, int buf, int ks) {
      const char* sb = smem + buf * G1_STAGE;
#pragma unroll
      for (int kk = 0; kk < 2; ++kk) {
        const int kq = 2 * ks + kk;
#pragma unroll
        for (int dt = 0; dt < 2; ++dt) {
          s16x4 lo = __builtin_amdgcn_ds_read_tr16_b64_v4i16((LDS_AS s16x4*)(sb + xrd[dt] + (16 * kq) * 256));
          s16x4 hi = __builtin_amdgcn_ds_read_tr16_b64_v4i16((LDS_AS s16x4*)(sb + xrd[dt] + (16 * kq + 4) * 256));
          f.xf[kk][dt] = __builtin_shufflevector(lo, hi, 0, 1, 2, 3, 4, 5, 6, 7);
        }
#pragma unroll
        for (int tt = 0; tt < 2; ++tt) f.cf[kk][tt] = *(const bf16x8*)(sb + crd[kq] + tt * 4096);
      }
    };
    auto mma = [&](const G1Slice& f) {
#pragma unroll
      for (int kk = 0; kk < 2; ++kk)
#pragma unroll
        for (int dt = 0; dt < 2; ++dt)
#pragma unroll
          for (int tt = 0; tt < 2; ++tt)
            acc[dt][tt] = __builtin_amdgcn_mfma_f32_32x32x16_bf16(f.xf[kk][dt], f.cf[kk][tt], acc[dt][tt], 0, 0, 0);
    };
#define ILV12_8() do { _Pragma("unroll") for (int _i = 0; _i < 4; ++_i) { SGB(0x8, 1); SGB(0x100, 3); } SGB(0x8, 4); } while (0)
    G1Slice f0, f1;
    BAR();
    load_slice(f0, 0, 0);
    int bk = 0;
    for (int k = 0; k < 31; ++k) {
      SCHED(); load_slice(f1, bk, 1); mma(f0); ILV12_8(); SCHED();
      WAIT_L();
      BAR();
      bk = nxt(bk);
      SCHED(); load_slice(f0, bk, 0); mma(f1); ILV12_8(); SCHED();
    }
    SCHED(); load_slice(f1, bk, 1); mma(f0); ILV12_8(); SCHED();
    mma(f1);
    WAIT_L();
    const float sgn = is_im ? -1.f : 1.f;
    char* ob = smem + 3 * G1_STAGE;
#pragma unroll
    for (int tt = 0; tt < 2; ++tt)
#pragma unroll
      for (int dt = 0; dt < 2; ++dt)
#pragma unroll
        for (int rq = 0; rq < 4; ++rq) {
          u32x2 v;
          v.x = pack2(sgn * acc[dt][tt][4 * rq + 0], sgn * acc[dt][tt][4 * rq + 1]);
          v.y = pack2(sgn * acc[dt][tt][4 * rq + 2], sgn * acc[dt][tt][4 * rq + 3]);
          *(u32x2*)(ob + (64 * wt + 32 * tt + r32) * 272 + (64 * wd + 32 * dt + 8 * rq + 4 * h) * 2) = v;
        }
  }
  __syncthreads();
  {
    const char* ob = smem + 3 * G1_STAGE;
    u16* outp = PQ + (size_t)trow0 * (2 * D_DIM) + (is_im ? D_DIM : 0) + d0;
#pragma unroll
    for (int i = 0; i < 3; ++i) {
      int row = 4 * (w + 12 * i) + g;
      if (row < 128) {
        u32x4 v = *(const u32x4*)(ob + row * 272 + r16 * 16);
        *(u32x4*)(outp + (size_t)row * (2 * D_DIM) + r16 * 8) = v;
      }
    }
  }
}

__device__ __forceinline__ void helper_block(int hb, const u16* __restrict__ Cre, const u16* __restrict__ Cim,
                                             const u16* __restrict__ x, const u16* __restrict__ q,
                                             const u16* __restrict__ k, const u16* __restrict__ v,
                                             const u16* __restrict__ shk, const u16* __restrict__ ssmk,
                                             const u16* __restrict__ Wre, const u16* __restrict__ Wim,
                                             u16* __restrict__ kvT) {
  const int w = __builtin_amdgcn_readfirstlane(threadIdx.x >> 6);
  const int lane = threadIdx.x & 63;
  const int xcd = hb & 7;
  {
    const int wi = (hb >> 3) * 12 + w;
    constexpr int NW = NHELP / 8 * 12;
#pragma unroll 1
    for (int u = wi; u < 32 * 18; u += NW) {
      const int step = u / 18, n = u - step * 18;
      const char* p;
      if (n < 8) {
        int row = 256 * xcd + 64 * (n & 3) + lane;
        const u16* cb = (n >= 4) ? Cim : Cre;
        p = (const char*)(cb + (size_t)row * S_LEN) + step * 128;
      } else {
        p = (const char*)x + (size_t)step * (64 * D_DIM * 2) + (n - 8) * 8192 + lane * 128;
      }
      touch(p);
    }
  }
  const int gw = w * NHELP + hb;
  if (gw >= 640) {
#pragma unroll 1
    for (int u = gw - 640; u < 840; u += 512) {
      const char* p;
      if (u < 320) p = (const char*)q + (size_t)u * 8192;
      else if (u < 640) p = (const char*)ssmk + (size_t)(u - 320) * 8192;
      else if (u < 740) p = (const char*)Wre + (size_t)(u - 640) * 8192;
      else p = (const char*)Wim + (size_t)(u - 740) * 8192;
      touch(p + lane * 128);
    }
  } else {
    const int s0 = (gw / 10) * 32, c0 = (gw % 10) * 64;
    const int rg = lane >> 3, cc = lane & 7;
    const int d = c0 + 8 * cc;
    const int r0 = s0 + 4 * rg;
    u32x4 kr[4], vr[4], kh[3];
#pragma unroll
    for (int r = 0; r < 4; ++r) kr[r] = *(const u32x4*)(k + (size_t)(r0 + r) * D_DIM + d);
#pragma unroll
    for (int j = 0; j < 3; ++j) {
      if (s0 == 0) kh[j] = (u32x4){0u, 0u, 0u, 0u};
      else kh[j] = *(const u32x4*)(k + (size_t)(s0 - 3 + j) * D_DIM + d);
    }
#pragma unroll
    for (int r = 0; r < 4; ++r) vr[r] = *(const u32x4*)(v + (size_t)(r0 + r) * D_DIM + d);
    u32x4 sk[4];
#pragma unroll
    for (int cp = 0; cp < 4; ++cp) sk[cp] = *(const u32x4*)(shk + (size_t)(d + 2 * cp) * 4);
#pragma unroll
    for (int j = 0; j < 3; ++j) {
      u32x4 t;
      t.x = __shfl_up(kr[1 + j].x, 8, 64);
      t.y = __shfl_up(kr[1 + j].y, 8, 64);
      t.z = __shfl_up(kr[1 + j].z, 8, 64);
      t.w = __shfl_up(kr[1 + j].w, 8, 64);
      if (rg != 0) kh[j] = t;
    }
    const int pos = (r0 & ~7) + 4 - (r0 & 7);
#pragma unroll
    for (int cp = 0; cp < 4; ++cp) {
      f32x2 s0v, s1v, s2v, s3v;
      s0v.x = bf2f(sk[cp].x & 0xffffu); s1v.x = bf2f(sk[cp].x >> 16); s2v.x = bf2f(sk[cp].y & 0xffffu); s3v.x = bf2f(sk[cp].y >> 16);
      s0v.y = bf2f(sk[cp].z & 0xffffu); s1v.y = bf2f(sk[cp].z >> 16); s2v.y = bf2f(sk[cp].w & 0xffffu); s3v.y = bf2f(sk[cp].w >> 16);
      f32x2 K[7];
#pragma unroll
      for (int j = 0; j < 3; ++j) K[j] = unpk(dw(kh[j], cp));
#pragma unroll
      for (int r = 0; r < 4; ++r) K[3 + r] = unpk(dw(kr[r], cp));
      unsigned o[4];
#pragma unroll
      for (int r = 0; r < 4; ++r) {
        f32x2 a = rn2(K[r] * s3v);
        a = rn2(a + rn2(K[r + 1] * s2v));
        a = rn2(a + rn2(K[r + 2] * s1v));
        a = rn2(a + rn2(K[r + 3] * s0v));
        o[r] = cvtpk(a * unpk(dw(vr[r], cp)));
      }
      u32x2 lo, hi;
      lo.x = __builtin_amdgcn_perm(o[2], o[3], 0x05040100u); hi.x = __builtin_amdgcn_perm(o[2], o[3], 0x07060302u);
      lo.y = __builtin_amdgcn_perm(o[0], o[1], 0x05040100u); hi.y = __builtin_amdgcn_perm(o[0], o[1], 0x07060302u);
      *(u32x2*)(kvT + (size_t)(d + 2 * cp) * S_LEN + pos) = lo;
      *(u32x2*)(kvT + (size_t)(d + 2 * cp + 1) * S_LEN + pos) = hi;
    }
  }
}

static constexpr int K1_SMEM = G1_NS * G1_STAGE;

__global__ void __launch_bounds__(768, 3)
k1_kernel(const u16* __restrict__ Cre, const u16* __restrict__ Cim, const u16* __restrict__ x,
          u16* __restrict__ PQ, const u16* __restrict__ q, const u16* __restrict__ k,
          const u16* __restrict__ v, const u16* __restrict__ shk, const u16* __restrict__ ssmk,
          const u16* __restrict__ Wre, const u16* __restrict__ Wim, u16* __restrict__ kvT) {
  __shared__ __attribute__((aligned(16))) char smem[K1_SMEM];
  const int b = blockIdx.x;
  if (b < NGEMM1) big_gemm(smem, b, Cre, Cim, x, PQ);
  else helper_block(b - NGEMM1, Cre, Cim, x, q, k, v, shk, ssmk, Wre, Wim, kvT);
}

static constexpr int NCONV = 160;
static constexpr int CV_XS = (256 + 2048) * 2;
static constexpr int CV_HC = 2080 * 2;
static constexpr int CV_WAVE = CV_XS + 2 * CV_HC;

__device__ __forceinline__ void conv_block(char* smem, int cb, const u16* __restrict__ q,
                                           const u16* __restrict__ kvT, const u16* __restrict__ ssmk,
                                           const u16* __restrict__ ssmD, u16* __restrict__ out,
                                           const u16* __restrict__ PQ, const u16* __restrict__ Wre,
                                           const u16* __restrict__ Wim) {
  const int tid = threadIdx.x;
  const int w = __builtin_amdgcn_readfirstlane(tid >> 6);
  const int lane = tid & 63;
  const int grp = (cb & 7) * 20 + (cb >> 3);
  const int d0 = grp * 4;
  const int r0 = 4 * tid;
  u32x2 qv[4];
  auto qload = [&](int i) { qv[i] = *(const u32x2*)(q + (size_t)(r0 + i) * D_DIM + d0); };

  if (w >= 4) {
    const int xcd = cb & 7;
    const int wi = (cb >> 3) * 8 + (w - 4);
#pragma unroll 1
    for (int u = wi; u < 20 * 14; u += 160) {
      const int step = u / 14, n = u - step * 14;
      const char* p;
      if (n < 4) p = (const char*)(PQ + (size_t)(256 * xcd + 64 * n + lane) * (2 * D_DIM)) + step * 128;
      else {
        const u16* wb = (step < 10) ? Wre : Wim;
        p = (const char*)(wb + (size_t)(64 * (n - 4) + lane) * D_DIM) + (step < 10 ? step : step - 10) * 128;
      }
      touch(p);
    }
    if (w < 8) {
#pragma unroll
      for (int i = 0; i < 4; ++i) qload(i);
    }
  } else {
    const float Dv = bf2f(ssmD[d0 + w]);
    char* xs = smem + w * CV_WAVE;
    char* hc0 = xs + CV_XS;
    {
      u32x4 xv[4], hv[4];
      const u16* xg = kvT + (size_t)(d0 + w) * S_LEN;
      const u16* hg = ssmk + (size_t)(d0 + w) * S_LEN;
#pragma unroll
      for (int i = 0; i < 4; ++i) {
        xv[i] = *(const u32x4*)(xg + 512 * i + 8 * lane);
        hv[i] = *(const u32x4*)(hg + 512 * i + 8 * lane);
      }
      char* hc1 = hc0 + CV_HC;
#pragma unroll
      for (int i = 0; i < 4; ++i) {
        *(u32x4*)(xs + (256 + 512 * i + 8 * lane) * 2) = xv[i];
        unsigned nx = __shfl_down(hv[i].x, 1, 64);
        unsigned nxt_first = (i < 3) ? __shfl(hv[(i < 3) ? i + 1 : 3].x, 0, 64) : 0u;
        if (lane == 63) nx = nxt_first;
        u32x4 sh;
        sh.x = __builtin_amdgcn_alignbyte(hv[i].y, hv[i].x, 2);
        sh.y = __builtin_amdgcn_alignbyte(hv[i].z, hv[i].y, 2);
        sh.z = __builtin_amdgcn_alignbyte(hv[i].w, hv[i].z, 2);
        sh.w = __builtin_amdgcn_alignbyte(nx, hv[i].w, 2);
        *(u32x4*)(hc0 + 32 + (512 * i + 8 * lane) * 2) = hv[i];
        *(u32x4*)(hc1 + 32 + (512 * i + 8 * lane) * 2) = sh;
      }
      unsigned h0 = __shfl(hv[0].x, 0, 64);
      if (lane < 2) *(u32x4*)(hc0 + 16 * lane) = (u32x4){0u, 0u, 0u, 0u};
      if (lane == 2) *(u32x4*)(hc1) = (u32x4){0u, 0u, 0u, 0u};
      if (lane == 3) *(u32x4*)(hc1 + 16) = (u32x4){0u, 0u, 0u, (h0 & 0xffffu) << 16};
      if (lane < 32) *(u32x4*)(xs + 16 * lane) = (u32x4){0u, 0u, 0u, 0u};
    }
    WAIT_L();

    const int g = lane >> 4, r16 = lane & 15;
    const char* abase = (r16 & 1) ? (hc0 + 2 * (25 + r16 - 8 * g)) : (hc0 + CV_HC + 2 * (24 + r16 - 8 * g));
    const char* bbase = xs + 2 * (240 + 16 * r16 + 8 * g);
    f32x4 acc[8];
#pragma unroll
    for (int i = 0; i < 8; ++i) acc[i] = (f32x4){0.f, 0.f, 0.f, 0.f};
    struct CvFrags { bf16x8 af[8], bfm[8]; };
    auto cv_load = [&](CvFrags& f, int rho) {
#pragma unroll
      for (int u = 0; u < 8; ++u) {
        const unsigned* ap = (const unsigned*)(abase + 64 * (8 * u) + 64 * rho);
        u32x4 t;
        t.x = ap[0]; t.y = ap[1]; t.z = ap[2]; t.w = ap[3];
        f.af[u] = __builtin_bit_cast(bf16x8, t);
      }
#pragma unroll
      for (int J = 0; J < 8; ++J) f.bfm[J] = *(const bf16x8*)(bbase + 512 * J - 64 * rho);
    };
    auto cv_mma = [&](const CvFrags& f) {
#pragma unroll
      for (int J = 0; J < 8; ++J)
#pragma unroll
        for (int I = J; I < 8; ++I)
          acc[I] = __builtin_amdgcn_mfma_f32_16x16x32_bf16(f.af[I - J], f.bfm[J], acc[I], 0, 0, 0);
    };
#define ILV24_36() do { _Pragma("unroll") for (int _i = 0; _i < 24; ++_i) { SGB(0x100, 1); SGB(0x8, 1); } SGB(0x8, 12); } while (0)
    {
      CvFrags ca, cb2;
      cv_load(ca, 0);
#pragma unroll
      for (int rho = 0; rho < 6; rho += 2) {
        qload(rho >> 1);
        SCHED(); cv_load(cb2, rho + 1); cv_mma(ca); ILV24_36(); SCHED();
        SCHED(); cv_load(ca, rho + 2); cv_mma(cb2); ILV24_36(); SCHED();
      }
      qload(3);
      SCHED(); cv_load(cb2, 7); cv_mma(ca); ILV24_36(); SCHED();
      cv_mma(cb2);
    }

    {
      const f32x2 D2 = {Dv, Dv};
#pragma unroll
      for (int I = 0; I < 8; ++I) {
        int n = 256 * I + 16 * r16 + 4 * g;
        u32x2 kvp = *(const u32x2*)(xs + 2 * (256 + (n & ~7) + 4 - (n & 7)));
        f32x2 a01 = {acc[I][0], acc[I][1]}, a23 = {acc[I][2], acc[I][3]};
        u32x2 o;
        o.x = cvtpk(rn2(a01) + rn2(unpk_sw(kvp.y) * D2));
        o.y = cvtpk(rn2(a23) + rn2(unpk_sw(kvp.x) * D2));
        *(u32x2*)(hc0 + 2 * n) = o;
      }
    }
  }
  __syncthreads();

  if (w < 8) {
    u32x2 zc[4];
#pragma unroll
    for (int c = 0; c < 4; ++c) zc[c] = *(const u32x2*)(smem + c * CV_WAVE + CV_XS + 2 * r0);
#pragma unroll
    for (int r = 0; r < 4; ++r) {
      unsigned o[2];
#pragma unroll
      for (int cp = 0; cp < 2; ++cp) {
        unsigned za = (r < 2) ? zc[2 * cp].x : zc[2 * cp].y;
        unsigned zb = (r < 2) ? zc[2 * cp + 1].x : zc[2 * cp + 1].y;
        f32x2 z;
        z.x = __uint_as_float((r & 1) ? (za & 0xffff0000u) : (za << 16));
        z.y = __uint_as_float((r & 1) ? (zb & 0xffff0000u) : (zb << 16));
        o[cp] = cvtpk(unpk(cp ? qv[r].y : qv[r].x) * z);
      }
      u32x2 ov = {o[0], o[1]};
      *(u32x2*)(out + (size_t)(r0 + r) * (2 * D_DIM) + d0) = ov;
    }
  }
}

static constexpr int G2_NS = 5;
static constexpr int G2_PT = 128 * 128;
static constexpr int G2_WT = 128 * 128;
static constexpr int G2_STAGE = G2_PT + G2_WT;
static constexpr int NGEMM2 = 80;

struct G2Slice {
  bf16x8 pf[2][2];
  bf16x8 wf[2][2];
};

__device__ __forceinline__ void small_gemm(char* smem, int b, const u16* __restrict__ PQ,
                                           const u16* __restrict__ Wre, const u16* __restrict__ Wim,
                                           u16* __restrict__ out) {
  const int tid = threadIdx.x;
  const int w = __builtin_amdgcn_readfirstlane(tid >> 6);
  const int lane = tid & 63;
  const int xcd = b & 7, idx = b >> 3;
  const int ttile = xcd * 2 + idx / 5;
  const int etile = idx % 5;
  const int t0 = ttile * 128, e0 = etile * 128;
  auto nxt = [](int bb) { return (bb + 1 == G2_NS) ? 0 : bb + 1; };
  const int g = lane >> 4, r16 = lane & 15;

  if (w >= 4) {
    const int lw = w - 4;
    const unsigned lds0 = __builtin_amdgcn_readfirstlane((unsigned)(size_t)smem);
    unsigned poff[2], woff[2];
#pragma unroll
    for (int i = 0; i < 2; ++i) {
      int row = 16 * lw + 8 * i + (lane >> 3);
      int sc = (lane & 7) ^ ((row >> 1) & 7);
      poff[i] = ((t0 + row) * (2 * D_DIM) + 8 * sc) * 2;
      woff[i] = ((e0 + row) * D_DIM + 8 * sc) * 2;
    }
    auto stage_issue = [&](int step, int buf) {
      unsigned sb = lds0 + buf * G2_STAGE;
      const u16* pg = PQ + step * 64;
      const u16* wg = (step < 10) ? (Wre + step * 64) : (Wim + (step - 10) * 64);
#pragma unroll
      for (int i = 0; i < 2; ++i) {
        glds16(pg, poff[i], sb + (16 * lw + 8 * i) * 128);
        glds16(wg, woff[i], sb + G2_PT + (16 * lw + 8 * i) * 128);
      }
    };
    stage_issue(0, 0);
    stage_issue(1, 1);
    WAIT_V(4);
    BAR();
#pragma unroll
    for (int s = 2; s < G2_NS; ++s) stage_issue(s, s);
    int bk = 0;
    for (int k = 0; k < 15; ++k) {
      WAIT_V(12);
      BAR();
      stage_issue(k + G2_NS, bk);
      bk = nxt(bk);
    }
    WAIT_V(12); BAR();
    WAIT_V(8);  BAR();
    WAIT_V(4);  BAR();
    WAIT_V(0);  BAR();
  } else {
    const int wt = w >> 1, we = w & 1;
    const int r32 = lane & 31, h = lane >> 5;
    int prd[4], wrd[4];
#pragma unroll
    for (int kq = 0; kq < 4; ++kq) {
      int sw = 16 * ((2 * kq + h) ^ ((r32 >> 1) & 7));
      prd[kq] = (64 * wt + r32) * 128 + sw;
      wrd[kq] = G2_PT + (64 * we + r32) * 128 + sw;
    }
    f32x16 acc[2][2];
#pragma unroll
    for (int i = 0; i < 2; ++i)
#pragma unroll
      for (int j = 0; j < 2; ++j)
#pragma unroll
        for (int e = 0; e < 16; ++e) acc[i][j][e] = 0.f;
    auto load_slice = [&](G2Slice& f, int buf, int ks) {
      const char* sb = smem + buf * G2_STAGE;
#pragma unroll
      for (int kk = 0; kk < 2; ++kk)
#pragma unroll
        for (int i = 0; i < 2; ++i) {
          f.wf[kk][i] = *(const bf16x8*)(sb + wrd[2 * ks + kk] + i * 4096);
          f.pf[kk][i] = *(const bf16x8*)(sb + prd[2 * ks + kk] + i * 4096);
        }
    };
    auto mma = [&](const G2Slice& f) {
#pragma unroll
      for (int kk = 0; kk < 2; ++kk)
#pragma unroll
        for (int et = 0; et < 2; ++et)
#pragma unroll
          for (int tt = 0; tt < 2; ++tt)
            acc[et][tt] = __builtin_amdgcn_mfma_f32_32x32x16_bf16(f.wf[kk][et], f.pf[kk][tt], acc[et][tt], 0, 0, 0);
    };
#define ILV8_8() do { _Pragma("unroll") for (int _i = 0; _i < 4; ++_i) { SGB(0x8, 1); SGB(0x100, 2); } SGB(0x8, 4); } while (0)
    G2Slice f0, f1;
    BAR();
    load_slice(f0, 0, 0);
    int bk = 0;
    for (int k = 0; k < 19; ++k) {
      SCHED(); load_slice(f1, bk, 1); mma(f0); ILV8_8(); SCHED();
      WAIT_L();
      BAR();
      bk = nxt(bk);
      SCHED(); load_slice(f0, bk, 0); mma(f1); ILV8_8(); SCHED();
    }
    SCHED(); load_slice(f1, bk, 1); mma(f0); ILV8_8(); SCHED();
    mma(f1);
    WAIT_L();
    char* ob = smem;
#pragma unroll
    for (int et = 0; et < 2; ++et)
#pragma unroll
      for (int tt = 0; tt < 2; ++tt)
#pragma unroll
        for (int rq = 0; rq < 4; ++rq) {
          u32x2 v;
          v.x = pack2(acc[et][tt][4 * rq + 0], acc[et][tt][4 * rq + 1]);
          v.y = pack2(acc[et][tt][4 * rq + 2], acc[et][tt][4 * rq + 3]);
          *(u32x2*)(ob + (64 * wt + 32 * tt + r32) * 272 + (64 * we + 32 * et + 8 * rq + 4 * h) * 2) = v;
        }
  }
  __syncthreads();
  {
    const char* ob = smem;
    u16* op = out + (size_t)t0 * (2 * D_DIM) + D_DIM + e0;
#pragma unroll
    for (int i = 0; i < 3; ++i) {
      int row = 4 * (w + 12 * i) + g;
      if (row < 128) {
        u32x4 v = *(const u32x4*)(ob + row * 272 + r16 * 16);
        *(u32x4*)(op + (size_t)row * (2 * D_DIM) + r16 * 8) = v;
      }
    }
  }
}

static constexpr int K2_SMEM = G2_NS * G2_STAGE;
static_assert(K2_SMEM >= 4 * CV_WAVE, "lds");

__global__ void __launch_bounds__(768, 3)
k2_kernel(const u16* __restrict__ PQ, const u16* __restrict__ Wre, const u16* __restrict__ Wim,
          const u16* __restrict__ q, const u16* __restrict__ kvT, const u16* __restrict__ ssmk,
          const u16* __restrict__ ssmD, u16* __restrict__ out) {
  __shared__ __attribute__((aligned(16))) char smem[K2_SMEM];
  const int b = blockIdx.x;
  if (b < NGEMM2) small_gemm(smem, b, PQ, Wre, Wim, out);
  else conv_block(smem, b - NGEMM2, q, kvT, ssmk, ssmD, out, PQ, Wre, Wim);
}

extern "C" void kernel_launch(void* const* d_in, const int* in_sizes, int n_in, void* d_out,
                              int out_size, void* d_ws, size_t ws_size, hipStream_t stream) {
  const u16* q = (const u16*)d_in[0];
  const u16* k = (const u16*)d_in[1];
  const u16* v = (const u16*)d_in[2];
  const u16* shk = (const u16*)d_in[3];
  const u16* ssmk = (const u16*)d_in[4];
  const u16* ssmD = (const u16*)d_in[5];
  const u16* x = (const u16*)d_in[6];
  const u16* Cre = (const u16*)d_in[7];
  const u16* Cim = (const u16*)d_in[8];
  const u16* Wre = (const u16*)d_in[9];
  const u16* Wim = (const u16*)d_in[10];
  u16* out = (u16*)d_out;
  u16* PQ = (u16*)d_ws;
  u16* kvT = PQ + (size_t)S_LEN * 2 * D_DIM;

  k1_kernel<<<NGEMM1 + NHELP, 768, 0, stream>>>(Cre, Cim, x, PQ, q, k, v, shk, ssmk, Wre, Wim, kvT);
  k2_kernel<<<NCONV + NGEMM2, 768, 0, stream>>>(PQ, Wre, Wim, q, kvT, ssmk, ssmD, out);
}
```
